```python
import torch

def _softmax(x, axis=-1):
    return torch.softmax(x, dim=axis)
def _gelu(x):
    return torch.nn.functional.gelu(x, approximate="tanh")
def _silu(x):
    return torch.nn.functional.silu(x)
def _sigmoid(x):
    return torch.sigmoid(x)
def _layernorm(x, axis=-1, eps=1e-5):
    m = x.mean(dim=axis, keepdim=True)
    v = x.var(dim=axis, keepdim=True, unbiased=False)
    return (x - m) / torch.sqrt(v + eps)
def _scatter_add(tgt, idx, src, axis=0):
    return tgt.clone().index_add_(axis, idx, src)
def _segment_sum(src, seg, n):
    out = torch.zeros((n,) + src.shape[1:], dtype=src.dtype)
    return out.index_add_(0, seg, src)
def _pad(x, pw):
    flat = tuple(v for pair in reversed(pw) for v in pair)
    return torch.nn.functional.pad(x, flat)

kernel_name = 'parallel_sum__linear_kernel_hedgehog__glu_gated_glu__prompt'

def setup_inputs():

    _w = (lambda a: (lambda t: t.to(torch.bfloat16) if t.is_floating_point() else t)(torch.as_tensor(a)))
    def _A_setup():
        B, S, D, H = 1, 2048, 512, 32
        Dh = D // H
        return {
            "x": _w((lambda *s: torch.randn(*s, dtype=torch.bfloat16) * 0.05)(B, S, D)),
            "wqkv": _w((lambda *s: torch.randn(*s, dtype=torch.bfloat16) * 0.4)(D, 3 * D)),
            "w_phi": _w((lambda *s: torch.randn(*s, dtype=torch.bfloat16) * 0.4)(H, Dh, Dh) * (Dh ** -0.5)),
            "wo": _w((lambda *s: torch.randn(*s, dtype=torch.bfloat16) * 0.1)(D, D)),
        }
    _A_in = _A_setup()

    def _B_setup():
        B, S, D, F = 1, 2048, 512, 1792
        return {
            "x": _w((lambda *s: torch.randn(*s, dtype=torch.bfloat16) * 0.05)(B, S, D)),
            "w_gate": _w((lambda *s: torch.randn(*s, dtype=torch.bfloat16) * 0.05)(D, F)),
            "w_up": _w((lambda *s: torch.randn(*s, dtype=torch.bfloat16) * 0.05)(D, F)),
            "w_down": _w((lambda *s: torch.randn(*s, dtype=torch.bfloat16) * 0.05)(F, D)),
        }
    _B_in = _B_setup()

    return {
        "A_x": _A_in["x"],
        "A_wqkv": _A_in["wqkv"],
        "A_w_phi": _A_in["w_phi"],
        "A_wo": _A_in["wo"],
        "B_x": _B_in["x"],
        "B_w_gate": _B_in["w_gate"],
        "B_w_up": _B_in["w_up"],
        "B_w_down": _B_in["w_down"],
    }

def reference(A_x, A_wqkv, A_w_phi, A_wo, B_x, B_w_gate, B_w_up, B_w_down):
    def _A_ref(x, wqkv, w_phi, wo):
        B, S, D = x.shape
        H = 32; Dh = D // H
        qkv = (lambda x, *d: x.permute(*d))((x @ wqkv).reshape(B, S, 3, H, Dh), 2, 0, 3, 1, 4)
        q, k, v = qkv[0], qkv[1], qkv[2]
        pq = torch.einsum("bhsd,hdf->bhsf", q, w_phi)
        pk = torch.einsum("bhsd,hdf->bhsf", k, w_phi)
        phi_q = torch.concatenate([_softmax(pq, axis=-1), _softmax(-pq, axis=-1)], axis=-1)
        phi_k = torch.concatenate([_softmax(pk, axis=-1), _softmax(-pk, axis=-1)], axis=-1)
        kv = torch.einsum("bhsf,bhsd->bhfd", phi_k, v)
        z  = torch.einsum("bhsf->bhf", phi_k)
        num = torch.einsum("bhqf,bhfd->bhqd", phi_q, kv)
        den = torch.einsum("bhqf,bhf->bhq", phi_q, z)[..., None] + 1e-6
        o = num / den
        return (lambda x, *d: x.permute(*d))(o, 0, 2, 1, 3).reshape(B, S, D) @ wo

    def _B_ref(x, w_gate, w_up, w_down):
        g = torch.einsum("bsd,df->bsf", x, w_gate)
        u = torch.einsum("bsd,df->bsf", x, w_up)
        h = _sigmoid(g) * u
        return torch.einsum("bsf,fd->bsd", h, w_down)

    x = A_x
    xn = _layernorm(x, axis=-1)
    return x + _A_ref(xn, A_wqkv, A_w_phi, A_wo) + _B_ref(xn, B_w_gate, B_w_up, B_w_down)
```

```cpp
#include <hip/hip_runtime.h>
#include <hip/hip_bf16.h>
typedef __hip_bfloat16 bf16;
constexpr int S=2048,D=512,H=32,DH=16,F=1792;
__device__ inline float b2f(bf16 x){return __bfloat162float(x);}
__device__ inline float rb(float x){return __bfloat162float(__float2bfloat16(x));}

__global__ void ln_k(const bf16* x, bf16* xn){
  int r=blockIdx.x; __shared__ float s[256];
  float a=b2f(x[r*D+threadIdx.x]), b=b2f(x[r*D+256+threadIdx.x]);
  s[threadIdx.x]=a+b; __syncthreads();
  for(int o=128;o>0;o>>=1){ if(threadIdx.x<o) s[threadIdx.x]+=s[threadIdx.x+o]; __syncthreads(); }
  float m=s[0]/D; __syncthreads();
  s[threadIdx.x]=(a-m)*(a-m)+(b-m)*(b-m); __syncthreads();
  for(int o=128;o>0;o>>=1){ if(threadIdx.x<o) s[threadIdx.x]+=s[threadIdx.x+o]; __syncthreads(); }
  float v=s[0]/D; float rs=rsqrtf(v+1e-5f);
  xn[r*D+threadIdx.x]=__float2bfloat16((a-m)*rs);
  xn[r*D+256+threadIdx.x]=__float2bfloat16((b-m)*rs);
}
__global__ void gemm_k(const bf16* A,const bf16* B,float* C,int M,int N,int K){
  __shared__ float sa[16][17], sb[16][17];
  int tx=threadIdx.x, ty=threadIdx.y; int r=blockIdx.y*16+ty, c=blockIdx.x*16+tx; float acc=0;
  for(int k=0;k<K;k+=16){ sa[ty][tx]=b2f(A[r*K+k+tx]); sb[ty][tx]=b2f(B[(k+ty)*N+c]); __syncthreads();
    for(int j=0;j<16;j++) acc+=sa[ty][j]*sb[j][tx]; __syncthreads(); }
  C[r*N+c]=acc;
}
__global__ void phi_k(const float* qkv,const bf16* wphi,float* phiq,float* phik){
  int idx=blockIdx.x*blockDim.x+threadIdx.x;
  if(idx>=S*H*2) return; int w=idx&1, h=(idx>>1)%H, s=idx/(2*H);
  float p[16]; float mx=-1e30f, mn=1e30f;
  for(int f=0;f<16;f++){ float a=0; for(int d=0;d<16;d++) a+=rb(qkv[s*1536+w*512+h*16+d])*b2f(wphi[h*256+d*16+f]); a=rb(a); p[f]=a; mx=fmaxf(mx,a); mn=fminf(mn,a);}
  float s1=0,s2=0; for(int f=0;f<16;f++){ s1+=__expf(p[f]-mx); s2+=__expf(mn-p[f]); }
  float* o=(w?phik:phiq)+(h*S+s)*32;
  for(int f=0;f<16;f++){ o[f]=rb(__expf(p[f]-mx)/s1); o[16+f]=rb(__expf(mn-p[f])/s2);}
}
__global__ void kv_k(const float* qkv,const float* phik,float* kv){
  int h=blockIdx.x; int t=threadIdx.x; if(t>=32*17) return; int f=t/17,d=t%17; float a=0;
  for(int s=0;s<S;s++){ float v= d<16? rb(qkv[s*1536+1024+h*16+d]):1.f; a+=phik[(h*S+s)*32+f]*v; }
  kv[h*544+t]=rb(a);
}
__global__ void o_k(const float* phiq,const float* kv,bf16* o){
  int idx=blockIdx.x*blockDim.x+threadIdx.x; int d=idx%16,h=(idx/16)%H,s=idx/512;
  float n=0,de=0; for(int f=0;f<32;f++){ float p=phiq[(h*S+s)*32+f]; n+=p*kv[h*544+f*17+d]; de+=p*kv[h*544+f*17+16]; }
  o[idx]=__float2bfloat16(rb(n)/rb(rb(de)+1e-6f));
}
__global__ void h_k(const float* g,const float* u,bf16* h){ int i=blockIdx.x*blockDim.x+threadIdx.x; float gg=rb(g[i]); float sg=rb(1.f/(1.f+__expf(-gg))); h[i]=__float2bfloat16(sg*rb(u[i])); }
__global__ void fin_k(const bf16* x,const float* a,const float* b,bf16* out){ int i=blockIdx.x*blockDim.x+threadIdx.x; out[i]=__float2bfloat16(rb(b2f(x[i])+rb(a[i]))+rb(b[i])); }

extern "C" void kernel_launch(void* const* d_in, const int* in_sizes, int n_in, void* d_out, int out_size, void* d_ws, size_t ws_size, hipStream_t stream) {
  const bf16* x=(const bf16*)d_in[0]; const bf16* wqkv=(const bf16*)d_in[1]; const bf16* wphi=(const bf16*)d_in[2]; const bf16* wo=(const bf16*)d_in[3];
  const bf16* wg=(const bf16*)d_in[5]; const bf16* wu=(const bf16*)d_in[6]; const bf16* wd=(const bf16*)d_in[7];
  char* ws=(char*)d_ws; size_t o=0; auto al=[&](size_t n){void* p=ws+o; o+=(n+255)&~size_t(255); return p;};
  bf16* xn=(bf16*)al(S*D*2); float* qkv=(float*)al(S*1536*4); float* phiq=(float*)al(H*S*32*4); float* phik=(float*)al(H*S*32*4);
  float* kv=(float*)al(H*544*4); bf16* ob=(bf16*)al(S*D*2); float* g=(float*)al(S*F*4); float* u=(float*)al(S*F*4); bf16* hb=(bf16*)al(S*F*2);
  float* a=(float*)al(S*D*4); float* b=(float*)al(S*D*4);
  ln_k<<<S,256,0,stream>>>(x,xn);
  gemm_k<<<dim3(1536/16,S/16),dim3(16,16),0,stream>>>(xn,wqkv,qkv,S,1536,D);
  gemm_k<<<dim3(F/16,S/16),dim3(16,16),0,stream>>>(xn,wg,g,S,F,D);
  gemm_k<<<dim3(F/16,S/16),dim3(16,16),0,stream>>>(xn,wu,u,S,F,D);
  phi_k<<<S*H*2/256,256,0,stream>>>(qkv,wphi,phiq,phik);
  kv_k<<<H,576,0,stream>>>(qkv,phik,kv);
  o_k<<<S*D/256,256,0,stream>>>(phiq,kv,ob);
  h_k<<<S*F/256,256,0,stream>>>(g,u,hb);
  gemm_k<<<dim3(D/16,S/16),dim3(16,16),0,stream>>>(ob,wo,a,S,D,D);
  gemm_k<<<dim3(D/16,S/16),dim3(16,16),0,stream>>>(hb,wd,b,S,D,F);
  fin_k<<<S*D/256,256,0,stream>>>(x,a,b,(bf16*)d_out);
}
```

```cpp
#include <hip/hip_runtime.h>
#include <stdint.h>

typedef __attribute__((ext_vector_type(8))) __bf16 b16x8;
typedef __attribute__((ext_vector_type(2))) __bf16 b16x2;
typedef __attribute__((ext_vector_type(8))) short s16x8;
typedef __attribute__((ext_vector_type(4))) short s16x4;
typedef __attribute__((ext_vector_type(4))) float f32x4;
typedef __attribute__((ext_vector_type(2))) float f32x2;
typedef __attribute__((ext_vector_type(16))) float f32x16;
typedef __attribute__((ext_vector_type(4))) unsigned u32x4;
typedef __attribute__((ext_vector_type(2))) unsigned u32x2;
typedef unsigned long long u64;
#define LAS __attribute__((address_space(3)))
#define GAS __attribute__((address_space(1)))
#define DI __device__ __forceinline__

#define STAMPS 0

constexpr unsigned WS_NONCE = 0;
constexpr unsigned WS_FKV = 1024;
constexpr unsigned WS_FH = 16384;
constexpr unsigned WS_DBG = 49152;
constexpr unsigned WS_KVP = 131072;
constexpr unsigned WS_ZP = WS_KVP + 8 * 4 * 16 * 1024;
constexpr unsigned WS_AB = 1u << 20;
constexpr unsigned WS_END = WS_AB + 64u * 144u * 1024u;
constexpr int WSLOT = 20480, NWS = 5, XOFF = 102400, XSLOT = 16384;
constexpr int SCR = 147456, LDS_BYTES = 163840;
constexpr float LOG2E = 1.4426950408889634f;
constexpr unsigned SPIN_CAP = 1u << 15;

extern __shared__ __attribute__((aligned(16))) char smem[];

DI unsigned pk2(float a, float b) { f32x2 f = {a, b}; return __builtin_bit_cast(unsigned, __builtin_convertvector(f, b16x2)); }
DI float lo16(unsigned u) { return __builtin_bit_cast(float, u << 16); }
DI float hi16(unsigned u) { return __builtin_bit_cast(float, u & 0xffff0000u); }
DI unsigned norm2(unsigned x, float a, float b) {
  unsigned o, t;
  asm("v_lshlrev_b32 %0, 16, %2\n\tv_and_b32 %1, 0xffff0000, %2\n\tv_fma_f32 %0, %0, %3, %4\n\tv_fma_f32 %1, %1, %3, %4\n\tv_cvt_pk_bf16_f32 %0, %0, %1"
      : "=&v"(o), "=&v"(t) : "v"(x), "v"(a), "v"(b));
  return o;
}
DI f32x4 mfma16(u32x4 a, u32x4 b, f32x4 c) {
  return __builtin_amdgcn_mfma_f32_16x16x32_bf16(__builtin_bit_cast(b16x8, a), __builtin_bit_cast(b16x8, b), c, 0, 0, 0);
}
DI f32x4 mfma16k16(u32x2 a, u32x2 b, f32x4 c) {
  return __builtin_amdgcn_mfma_f32_16x16x16bf16_1k(__builtin_bit_cast(s16x4, a), __builtin_bit_cast(s16x4, b), c, 0, 0, 0);
}
DI f32x16 mfma32(u32x4 a, u32x4 b, f32x16 c) {
  return __builtin_amdgcn_mfma_f32_32x32x16_bf16(__builtin_bit_cast(b16x8, a), __builtin_bit_cast(b16x8, b), c, 0, 0, 0);
}
DI u32x2 tr8(int off) {
  return __builtin_bit_cast(u32x2, __builtin_amdgcn_ds_read_tr16_b64_v4i16((LAS s16x4*)(smem + off)));
}
DI u32x4 tr16(int off, int second) { u32x2 a = tr8(off), b = tr8(off + second); return (u32x4){a[0], a[1], b[0], b[1]}; }
DI u32x4 lds_r128(int off) { return *(LAS u32x4*)(smem + off); }
DI void lds_w128(int off, u32x4 v) { *(LAS u32x4*)(smem + off) = v; }
DI void lds_w64(int off, u32x2 v) { *(LAS u32x2*)(smem + off) = v; }
DI void dma_s(unsigned voff, const char* sbase, unsigned lds) {
  asm volatile("s_mov_b32 m0, %2\n\ts_nop 0\n\tglobal_load_lds_dwordx4 %0, %1" ::"v"(voff), "s"(sbase), "s"(lds) : "memory");
}
DI void dma_v(const char* vaddr, unsigned lds) {
  asm volatile("s_mov_b32 m0, %1\n\ts_nop 0\n\tglobal_load_lds_dwordx4 %0, off" ::"v"(vaddr), "s"(lds) : "memory");
}
template <int N> DI void wait_vm() { asm volatile("s_waitcnt vmcnt(%0)" ::"i"(N) : "memory"); }
template <int N> DI void wait_vm_bar() { asm volatile("s_waitcnt vmcnt(%0) lgkmcnt(0)\n\ts_barrier" ::"i"(N) : "memory"); }
DI void bar() { asm volatile("s_waitcnt lgkmcnt(0)\n\ts_barrier" ::: "memory"); }
DI float fbits(unsigned u) { return __builtin_bit_cast(float, u); }
DI unsigned ubits(float f) { return __builtin_bit_cast(unsigned, f); }
DI float xmax(float v) {
  auto r = __builtin_amdgcn_permlane16_swap(ubits(v), ubits(v), false, false); v = fmaxf(fbits(r[0]), fbits(r[1]));
  r = __builtin_amdgcn_permlane32_swap(ubits(v), ubits(v), false, false); return fmaxf(fbits(r[0]), fbits(r[1]));
}
DI float xmin(float v) {
  auto r = __builtin_amdgcn_permlane16_swap(ubits(v), ubits(v), false, false); v = fminf(fbits(r[0]), fbits(r[1]));
  r = __builtin_amdgcn_permlane32_swap(ubits(v), ubits(v), false, false); return fminf(fbits(r[0]), fbits(r[1]));
}
DI float xsum(float v) {
  auto r = __builtin_amdgcn_permlane16_swap(ubits(v), ubits(v), false, false); v = fbits(r[0]) + fbits(r[1]);
  r = __builtin_amdgcn_permlane32_swap(ubits(v), ubits(v), false, false); return fbits(r[0]) + fbits(r[1]);
}
template <int CTRL> DI float dpp_add(float v) {
  return v + fbits(__builtin_amdgcn_update_dpp(0u, ubits(v), CTRL, 0xf, 0xf, true));
}
DI u32x4 dual_softmax(f32x4 p) {
  const float mx = xmax(fmaxf(fmaxf(p[0], p[1]), fmaxf(p[2], p[3])));
  const float mn = xmin(fminf(fminf(p[0], p[1]), fminf(p[2], p[3])));
  const float a = -mx * LOG2E, b = mn * LOG2E;
  float ep[4], em[4];
#pragma unroll
  for (int r = 0; r < 4; ++r) {
    ep[r] = __builtin_amdgcn_exp2f(__builtin_fmaf(p[r], LOG2E, a));
    em[r] = __builtin_amdgcn_exp2f(__builtin_fmaf(p[r], -LOG2E, b));
  }
  const float rp = __builtin_amdgcn_rcpf(xsum((ep[0] + ep[1]) + (ep[2] + ep[3])));
  const float rm = __builtin_amdgcn_rcpf(xsum((em[0] + em[1]) + (em[2] + em[3])));
  return (u32x4){pk2(ep[0] * rp, ep[1] * rp), pk2(ep[2] * rp, ep[3] * rp), pk2(em[0] * rm, em[1] * rm), pk2(em[2] * rm, em[3] * rm)};
}
DI u32x2 glu4(f32x4 g, f32x4 u) {
  float h[4];
#pragma unroll
  for (int r = 0; r < 4; ++r) h[r] = u[r] * __builtin_amdgcn_rcpf(1.0f + __builtin_amdgcn_exp2f(-LOG2E * g[r]));
  return (u32x2){pk2(h[0], h[1]), pk2(h[2], h[3])};
}
DI u32x4 mk_frag(u32x2 t0, u32x2 t1) {
  auto r0 = __builtin_amdgcn_permlane16_swap(t0[0], t1[0], false, false);
  auto r1 = __builtin_amdgcn_permlane16_swap(t0[1], t1[1], false, false);
  return (u32x4){r0[0], r1[0], r0[1], r1[1]};
}
DI void poll_flags(const char* ws, unsigned off, int n, u64 tag, int lane) {
  GAS u64* f = (GAS u64*)(ws + off) + (lane < n ? lane : 0);
#pragma unroll 1
  for (unsigned spins = 0; spins < SPIN_CAP; ++spins) {
    const u64 v = __hip_atomic_load(f, __ATOMIC_RELAXED, __HIP_MEMORY_SCOPE_AGENT);
    if (__all(v == tag)) break;
    __builtin_amdgcn_s_sleep(2);
  }
  asm volatile("" ::: "memory");
}
DI void set_flag(char* ws, unsigned off, u64 tag, int lane) {
  if (lane == 0) __hip_atomic_store((GAS u64*)(ws + off), tag, __ATOMIC_RELAXED, __HIP_MEMORY_SCOPE_AGENT);
}
#define STAMP(i)
#define STAMP_ARG
#define STAMP_PASS

struct Ptrs { const char *x, *wqkv, *wphi, *wo, *wg, *wu, *wd; char *out, *ws; };

DI void w2_dma(const Ptrs& P, int i0, int kq, int rh, int ct64, int lane) {
  const int r = lane >> 3;
  const unsigned voff = (unsigned)((r + 8 * rh) * 1024 + (((lane & 7) ^ (((r >> 1) & 1) << 2)) * 16) + ct64 * 128);
#pragma unroll
  for (int n = 0; n < 18; ++n) {
    const int i = i0 + n;
    const char* src = i < 16 ? P.wd + (256 * kq + 16 * i) * 1024
                    : i < 24 ? P.wd + (1024 + 128 * kq + 16 * (i - 16)) * 1024
                    : i < 28 ? P.wd + (1536 + 64 * kq + 16 * (i - 24)) * 1024
                             : P.wo + (128 * kq + 16 * (i - 28)) * 1024;
    dma_s(voff, src, kq * 36864 + n * 2048 + rh * 1024);
  }
}

template <bool TA>
DI void phase1(const Ptrs& P, int lane, int w, int p, int ab, int rot, int t64, int ct64, u64& tag_out STAMP_ARG) {
  const int wm = w >> 2, wn = w & 3;
  const int g = lane >> 4, t16 = lane & 15;
  u32x2 nonce; unsigned wp0, wp1, wp2, wp3;
  {
    const char* wsn = P.ws;
    const char* wph = P.wphi + (4 * ab + wn) * 512;
    const unsigned z = 0, vo = (unsigned)(g * 128 + t16 * 2);
    asm volatile("global_load_dwordx2 %0, %1, %2 sc1" : "=v"(nonce) : "v"(z), "s"(wsn) : "memory");
    asm volatile("global_load_ushort %0, %4, %5\n\tglobal_load_ushort %1, %4, %5 offset:32\n\t"
                 "global_load_ushort %2, %4, %5 offset:64\n\tglobal_load_ushort %3, %4, %5 offset:96"
                 : "=&v"(wp0), "=&v"(wp1), "=&v"(wp2), "=&v"(wp3) : "v"(vo), "s"(wph) : "memory");
  }
  u32x4 X[16];
  {
    const unsigned voff = (unsigned)((lane >> 3) * 1024 + (lane & 7) * 16);
    const char* xb = P.x + (p * 128 + 16 * w) * 1024;
#pragma unroll
    for (int i = 0; i < 16; ++i) {
      const char* s = xb + (((i >> 1) + rot) & 7) * 128 + (i & 1) * 8192;
      asm volatile("global_load_dwordx4 %0, %1, %2" : "=v"(X[i]) : "v"(voff), "s"(s) : "memory");
    }
  }
  const int oct = w & 3;
  const int dr = lane >> 3;
  const int dsw = (lane & 7) ^ ((((dr >> 1) & 1) | ((oct & 1) << 1)) << 1);
  const unsigned vq = (unsigned)((8 * oct + dr) * 3072 + dsw * 16), vg = (unsigned)((8 * oct + dr) * 3584 + dsw * 16);
  const char* gb[5];
  const char* hp = nullptr;
  if (TA) {
    gb[0] = P.wqkv + (512 + 64 * ab) * 2; gb[1] = P.wqkv + (1024 + 64 * ab) * 2;
    gb[2] = P.wg + (16 + ab) * 128; gb[3] = P.wu + (16 + ab) * 128; gb[4] = nullptr;
    hp = (dsw < 4 ? P.wg : P.wu) + (24 + (ab >> 1)) * 128 + (ab & 1) * 64 + (8 * oct + dr) * 3584 + (dsw & 3) * 16;
  } else {
    gb[0] = P.wqkv + 64 * ab * 2;
    gb[1] = P.wg + (2 * ab) * 128; gb[2] = P.wu + (2 * ab) * 128; gb[3] = P.wg + (2 * ab + 1) * 128; gb[4] = P.wu + (2 * ab + 1) * 128;
  }
  auto wpiece = [&](int G, int k0, unsigned slot) {
    const unsigned lds = slot + G * 4096 + oct * 1024;
    if (TA && G == 4) dma_v(hp + k0 * 3584, lds);
    else if (G == 0 || (TA && G == 1)) dma_s(vq, gb[G] + k0 * 3072, lds);
    else dma_s(vg, gb[G] + k0 * 3584, lds);
  };
  auto wstage = [&](int s) {
    const int k0 = (((s >> 1) + rot) & 7) * 64 + (s & 1) * 32;
    const unsigned slot = (s % NWS) * WSLOT;
    if (((s + wm) & 1) == 0) { wpiece(0, k0, slot); wpiece(1, k0, slot); wpiece(2, k0, slot); }
    else { wpiece(3, k0, slot); wpiece(4, k0, slot); }
  };
#pragma unroll
  for (int s = 0; s < NWS; ++s) wstage(s);
  STAMP(0);
  float s1[2] = {0.f, 0.f}, s2[2] = {0.f, 0.f};
  const b16x2 ones2 = __builtin_bit_cast(b16x2, 0x3f803f80u);
#define XWAIT(N, i) asm volatile("s_waitcnt vmcnt(%4)" : "+v"(X[i]), "+v"(X[i + 1]), "+v"(X[i + 2]), "+v"(X[i + 3]) : "i"(N) : "memory")
#pragma unroll
  for (int q = 0; q < 4; ++q) {
    if (q == 0) { XWAIT(24, 0); asm volatile("" : "+v"(nonce), "+v"(wp0), "+v"(wp1), "+v"(wp2), "+v"(wp3)); }
    if (q == 1) XWAIT(20, 4);
    if (q == 2) XWAIT(16, 8);
    if (q == 3) XWAIT(12, 12);
#pragma unroll
    for (int i = 4 * q; i < 4 * q + 4; ++i)
#pragma unroll
      for (int e = 0; e < 4; ++e) {
        const unsigned xv = X[i][e];
        const b16x2 v = __builtin_bit_cast(b16x2, xv);
        s1[i & 1] = __builtin_amdgcn_fdot2_f32_bf16(v, ones2, s1[i & 1], false);
        s2[i & 1] = __builtin_amdgcn_fdot2_f32_bf16(v, v, s2[i & 1], false);
      }
  }
  const u64 nn = ((u64)(unsigned)__builtin_amdgcn_readfirstlane(nonce[1]) << 32) | (unsigned)__builtin_amdgcn_readfirstlane(nonce[0]);
  const u64 tag = nn * 6364136223846793005ull + 1442695040888963407ull;
  tag_out = tag;
  const u32x2 wphi = {wp0 | (wp1 << 16), wp2 | (wp3 << 16)};
  float na[2], nb[2];
#pragma unroll
  for (int r = 0; r < 2; ++r) {
    float a = s1[r], b = s2[r];
    a = dpp_add<0xB1>(a); b = dpp_add<0xB1>(b);
    a = dpp_add<0x4E>(a); b = dpp_add<0x4E>(b);
    a = dpp_add<0x141>(a); b = dpp_add<0x141>(b);
    const float m = a * (1.0f / 512.0f);
    const float var = fmaxf(b * (1.0f / 512.0f) - m * m, 0.0f);
    na[r] = __builtin_amdgcn_rsqf(var + 1e-5f);
    nb[r] = -m * na[r];
  }
  const int xw = XOFF + (16 * w + dr) * 128 + (((lane & 7) ^ ((dr >> 1) & 3)) * 16);
  auto xwrite = [&](int i) {
    u32x4 o;
#pragma unroll
    for (int e = 0; e < 4; ++e)
      o[e] = norm2(X[i][e], na[i & 1], nb[i & 1]);
    lds_w128(((xw + (i & 1) * 1024) ^ ((i & 1) * 64)) + ((i >> 1) & 1) * XSLOT, o);
  };
  const int q4 = t16 >> 2, p4 = lane & 3;
  const int fsw = ((q4 >> 1) & 1) | ((g & 1) << 1);
  const int trb = g * 1024 + q4 * 128 + 8 * p4;
  const int tr0 = trb + ((wn ^ fsw) * 32);
  const int trg = trb + (((wn >> 1) ^ fsw) * 32) + 4 * 4096, tru = trb + (((2 + (wn >> 1)) ^ fsw) * 32) + 4 * 4096;
  const int xr = XOFF + (64 * wm + t16) * 128 + ((g ^ (t16 >> 1)) * 16);
  const int pr = TA ? (wn & 1) : 0;
  const int xrA = xr + pr * 4096, xrB = xr + (1 - pr) * 4096;
  constexpr int NWF = TA ? 6 : 5;
  u32x4 wf[2][NWF], xf[2][4];
  auto fread = [&](int s) {
    const int slot = (s % NWS) * WSLOT, b = s & 1;
#pragma unroll
    for (int G = 0; G < (TA ? 4 : 5); ++G) wf[b][G] = tr16(tr0 + slot + G * 4096, 512);
    if (TA) { wf[b][4] = tr16(trg + slot, 512); wf[b][5] = tr16(tru + slot, 512); }
    const int xs = ((s >> 1) & 1) * XSLOT;
    xf[b][0] = lds_r128((xrA ^ (b * 64)) + xs); xf[b][1] = lds_r128((xrA ^ (b * 64)) + xs + 2048);
    xf[b][2] = lds_r128((xrB ^ (b * 64)) + xs); xf[b][3] = lds_r128((xrB ^ (b * 64)) + xs + 2048);
  };
  f32x4 acc[5][4];
#pragma unroll
  for (int a = 0; a < 5; ++a)
#pragma unroll
    for (int b = 0; b < 4; ++b) acc[a][b] = (f32x4){0.f, 0.f, 0.f, 0.f};
  xwrite(0); xwrite(1);
  wait_vm_bar<9>();
  STAMP(1);
  fread(0);
#pragma unroll
  for (int s = 0; s < 16; ++s) {
    if (s < 15) {
      if (s <= 11) wait_vm_bar<7>(); else if (s == 12) wait_vm_bar<5>(); else if (s == 13) wait_vm_bar<2>(); else wait_vm_bar<0>();
      if (s + NWS < 16) wstage(s + NWS);
      if (s == 0) { xwrite(2); xwrite(3); }
      else if (s + 3 < 16) xwrite(s + 3);
      fread(s + 1);
    }
    const int b = s & 1;
#pragma unroll
    for (int G = 0; G < 4; ++G)
#pragma unroll
      for (int rt = 0; rt < 4; ++rt)
        acc[G][rt] = (TA && G == 1) ? mfma16(xf[b][rt], wf[b][G], acc[G][rt]) : mfma16(wf[b][G], xf[b][rt], acc[G][rt]);
    if (TA) {
      acc[4][0] = mfma16(wf[b][4], xf[b][0], acc[4][0]); acc[4][1] = mfma16(wf[b][4], xf[b][1], acc[4][1]);
      acc[4][2] = mfma16(wf[b][5], xf[b][0], acc[4][2]); acc[4][3] = mfma16(wf[b][5], xf[b][1], acc[4][3]);
    } else {
#pragma unroll
      for (int rt = 0; rt < 4; ++rt) acc[4][rt] = mfma16(wf[b][4], xf[b][rt], acc[4][rt]);
    }
  }
  bar();
  STAMP(2);
  const __amdgpu_buffer_rsrc_t wsr = __builtin_amdgcn_make_buffer_rsrc(P.ws, 0, WS_END, 0x00020000);
  const int kq2 = w & 3, rh2 = w >> 2;
  const unsigned abase = WS_AB + (unsigned)(p * 4 + wm * 2) * (144 * 1024);
  const unsigned l16 = lane * 16;
  const int scr = SCR + w * 2048;
  if (TA) {
    f32x4 kvp = {0.f, 0.f, 0.f, 0.f}, kvm = kvp, zp = kvp, zm = kvp;
    const int fw = (((t16 >> 2) & 1) << 2) | (((t16 >> 3) & 1) << 1) | ((t16 >> 1) & 1);
    const int rr = 4 * g + q4;
    const int fr = (((rr >> 2) & 1) << 2) | (((rr >> 3) & 1) << 1) | ((rr >> 1) & 1);
    const u32x4 ones = {0x3f803f80u, 0x3f803f80u, 0x3f803f80u, 0x3f803f80u};
#pragma unroll
    for (int pp = 0; pp < 2; ++pp) {
#pragma unroll
      for (int e = 0; e < 2; ++e) {
        const f32x4 k = acc[0][2 * pp + e];
        const f32x4 pk = mfma16k16(wphi, (u32x2){pk2(k[0], k[1]), pk2(k[2], k[3])}, (f32x4){0.f, 0.f, 0.f, 0.f});
        const u32x4 ph = dual_softmax(pk);
        lds_w64(scr + e * 1024 + t16 * 64 + ((g ^ fw) * 8), (u32x2){ph[0], ph[1]});
        lds_w64(scr + e * 1024 + t16 * 64 + (((4 + g) ^ fw) * 8), (u32x2){ph[2], ph[3]});
      }
      asm volatile("s_waitcnt lgkmcnt(0)" ::: "memory");
      const u32x4 ap = tr16(scr + rr * 64 + ((p4 ^ fr) * 8), 1024), am = tr16(scr + rr * 64 + (((4 + p4) ^ fr) * 8), 1024);
      const f32x4 v0 = acc[1][2 * pp], v1 = acc[1][2 * pp + 1];
      const u32x4 vb = {pk2(v0[0], v0[1]), pk2(v0[2], v0[3]), pk2(v1[0], v1[1]), pk2(v1[2], v1[3])};
      kvp = mfma16(ap, vb, kvp); kvm = mfma16(am, vb, kvm);
      zp = mfma16(ap, ones, zp); zm = mfma16(am, ones, zm);
      asm volatile("s_waitcnt lgkmcnt(0)" ::: "memory");
    }
    if (wm == 1) { lds_w128(scr + l16, __builtin_bit_cast(u32x4, kvp)); lds_w128(scr + 1024 + l16, __builtin_bit_cast(u32x4, kvm)); }
    if (t16 == 0)
      __builtin_amdgcn_raw_buffer_store_b128((u32x4){pk2(zp[0], zp[1]), pk2(zp[2], zp[3]), pk2(zm[0], zm[1]), pk2(zm[2], zm[3])}, wsr,
                                             g * 16, WS_ZP + (((ab * 4 + wn) * 2 + wm) * 16 + p) * 64, 16);
    bar();
    if (wm == 0) {
      kvp += __builtin_bit_cast(f32x4, lds_r128(scr + 4 * 2048 + l16));
      kvm += __builtin_bit_cast(f32x4, lds_r128(scr + 4 * 2048 + 1024 + l16));
      __builtin_amdgcn_raw_buffer_store_b128((u32x4){pk2(kvp[0], kvp[1]), pk2(kvp[2], kvp[3]), pk2(kvm[0], kvm[1]), pk2(kvm[2], kvm[3])}, wsr,
                                             l16, WS_KVP + ((ab * 4 + wn) * 16 + p) * 1024, 16);
    }
    w2_dma(P, 0, kq2, rh2, ct64, lane);
    wait_vm<18>();
    set_flag(P.ws, WS_FKV + ((ab * 4 + wn) * 32 + p * 2 + wm) * 8, tag, lane);
    STAMP(3);
#pragma unroll
    for (int pp = 0; pp < 2; ++pp) {
      const u32x4 fr4 = mk_frag(glu4(acc[2][2 * pp], acc[3][2 * pp]), glu4(acc[2][2 * pp + 1], acc[3][2 * pp + 1]));
      __builtin_amdgcn_raw_buffer_store_b128(fr4, wsr, l16, abase + (pp ^ pr) * (144 * 1024) + (64 + 4 * ab + wn) * 1024, 16);
    }
    {
      const u32x4 fr4 = mk_frag(glu4(acc[4][0], acc[4][2]), glu4(acc[4][1], acc[4][3]));
      __builtin_amdgcn_raw_buffer_store_b128(fr4, wsr, l16, abase + pr * (144 * 1024) + (96 + 2 * ab + (wn >> 1)) * 1024, 16);
    }
    wait_vm<0>();
    set_flag(P.ws, WS_FH + ((((p * 3 + 1) * 4 + (ab >> 1)) * 2 + wm) * 8 + (ab & 1) * 4 + wn) * 8, tag, lane);
    STAMP(4);
  } else {
    w2_dma(P, 0, kq2, rh2, ct64, lane);
#pragma unroll
    for (int h2 = 0; h2 < 2; ++h2)
#pragma unroll
      for (int pp = 0; pp < 2; ++pp) {
        const u32x4 fr4 = mk_frag(glu4(acc[1 + 2 * h2][2 * pp], acc[2 + 2 * h2][2 * pp]), glu4(acc[1 + 2 * h2][2 * pp + 1], acc[2 + 2 * h2][2 * pp + 1]));
        __builtin_amdgcn_raw_buffer_store_b128(fr4, wsr, l16, abase + pp * (144 * 1024) + (8 * ab + 4 * h2 + wn) * 1024, 16);
      }
    u32x4 ph[4];
#pragma unroll
    for (int rt = 0; rt < 4; ++rt) {
      const f32x4 q = acc[0][rt];
      ph[rt] = dual_softmax(mfma16k16(wphi, (u32x2){pk2(q[0], q[1]), pk2(q[2], q[3])}, (f32x4){0.f, 0.f, 0.f, 0.f}));
    }
    wait_vm<0>();
    set_flag(P.ws, WS_FH + ((((p * 3 + 0) * 4 + (ab >> 1)) * 2 + wm) * 8 + (ab & 1) * 4 + wn) * 8, tag, lane);
    STAMP(3);
    poll_flags(P.ws, WS_FKV + (ab * 4 + wn) * 256, 32, tag, lane);
    u32x4 part[8], zq[2];
#pragma unroll
    for (int i = 0; i < 8; ++i) part[i] = __builtin_amdgcn_raw_buffer_load_b128(wsr, l16, WS_KVP + ((ab * 4 + wn) * 16 + 8 * wm + i) * 1024, 16);
#pragma unroll
    for (int i = 0; i < 2; ++i) zq[i] = __builtin_amdgcn_raw_buffer_load_b128(wsr, t16 * 64 + g * 16, WS_ZP + ((ab * 4 + wn) * 2 + i) * 1024, 16);
    float kv[8], z[8];
#pragma unroll
    for (int e = 0; e < 4; ++e) {
      kv[2 * e] = lo16(part[0][e]); kv[2 * e + 1] = hi16(part[0][e]);
#pragma unroll
      for (int i = 1; i < 8; ++i) { kv[2 * e] += lo16(part[i][e]); kv[2 * e + 1] += hi16(part[i][e]); }
      z[2 * e] = lo16(zq[0][e]) + lo16(zq[1][e]); z[2 * e + 1] = hi16(zq[0][e]) + hi16(zq[1][e]);
    }
    lds_w128(scr + l16, (u32x4){ubits(kv[0]), ubits(kv[1]), ubits(kv[2]), ubits(kv[3])});
    lds_w128(scr + 1024 + l16, (u32x4){ubits(kv[4]), ubits(kv[5]), ubits(kv[6]), ubits(kv[7])});
#pragma unroll
    for (int e = 0; e < 8; ++e) {
      z[e] = dpp_add<0xB1>(z[e]); z[e] = dpp_add<0x4E>(z[e]); z[e] = dpp_add<0x141>(z[e]); z[e] = dpp_add<0x140>(z[e]);
    }
    bar();
    const f32x4 o0 = __builtin_bit_cast(f32x4, lds_r128((scr ^ (4 * 2048)) + l16)), o1 = __builtin_bit_cast(f32x4, lds_r128((scr ^ (4 * 2048)) + 1024 + l16));
    const u32x4 kvf = {pk2(kv[0] + o0[0], kv[1] + o0[1]), pk2(kv[2] + o0[2], kv[3] + o0[3]), pk2(kv[4] + o1[0], kv[5] + o1[1]), pk2(kv[6] + o1[2], kv[7] + o1[3])};
    const u32x4 zf = {pk2(z[0], z[1]), pk2(z[2], z[3]), pk2(z[4], z[5]), pk2(z[6], z[7])};
    u32x2 ot[4];
#pragma unroll
    for (int rt = 0; rt < 4; ++rt) {
      const f32x4 num = mfma16(kvf, ph[rt], (f32x4){0.f, 0.f, 0.f, 0.f});
      const f32x4 den = mfma16(zf, ph[rt], (f32x4){0.f, 0.f, 0.f, 0.f});
      const float r = __builtin_amdgcn_rcpf(den[0] + 1e-6f);
      ot[rt] = (u32x2){pk2(num[0] * r, num[1] * r), pk2(num[2] * r, num[3] * r)};
    }
#pragma unroll
    for (int pp = 0; pp < 2; ++pp)
      __builtin_amdgcn_raw_buffer_store_b128(mk_frag(ot[2 * pp], ot[2 * pp + 1]), wsr, l16, abase + pp * (144 * 1024) + (112 + 4 * ab + wn) * 1024, 16);
    wait_vm<0>();
    set_flag(P.ws, WS_FH + ((((p * 3 + 2) * 4 + (ab >> 1)) * 2 + wm) * 8 + (ab & 1) * 4 + wn) * 8, tag, lane);
    STAMP(4);
  }
}

__global__ __launch_bounds__(512, 2) void fused_block(const char* x, const char* wqkv, const char* wphi, const char* wo, const char* wg,
                                                      const char* wu, const char* wd, char* out, char* ws) {
  const int lane = threadIdx.x & 63;
  int w = __builtin_amdgcn_readfirstlane(threadIdx.x >> 6);
  const int b = blockIdx.x, xcd = b & 7, bi = b >> 3;
  const Ptrs P = {x, wqkv, wphi, wo, wg, wu, wd, out, ws};
  const int p = (xcd & 1) * 8 + (bi & 7), jj = bi >> 3, ab = (xcd >> 1) * 2 + (jj & 1);
  const int t64 = xcd * 4 + (bi >> 3), ct64 = bi & 7;
  STAMP(7);
  u64 tag;
  if (jj < 2) phase1<true>(P, lane, w, p, ab, bi & 7, t64, ct64, tag STAMP_PASS);
  else phase1<false>(P, lane, w, p, ab, bi & 7, t64, ct64, tag STAMP_PASS);

  asm volatile("" : "+s"(w));
  const int kq = w & 3, rh = w >> 2;
  const int h = lane >> 5, q4 = (lane & 15) >> 2, p4 = lane & 3, blk = (lane >> 4) & 1;
  const int w2r = kq * 36864 + (8 * h + q4) * 128 + 8 * p4;
  const int w2a = w2r + ((blk ^ (q4 & 2)) * 32), w2b = w2r + (((2 + blk) ^ (q4 & 2)) * 32);
  wait_vm_bar<0>();
  u32x4 wr[18][2];
#pragma unroll
  for (int i = 0; i < 18; ++i) { wr[i][0] = tr16(w2a + i * 2048, 512); wr[i][1] = tr16(w2b + i * 2048, 512); }
  bar();
  w2_dma(P, 18, kq, rh, ct64, lane);
  STAMP(5);
  const int p2 = t64 >> 1, wm2 = t64 & 1;
  const char* ab0 = ws + WS_AB + (unsigned)(t64 * 2 + rh) * (144 * 1024);
  const unsigned l16 = lane * 16;
  u32x4 res;
  {
    const char* xs = x + (t64 * 64 + rh * 32) * 1024 + ct64 * 128 + kq * 32;
    const unsigned vo = (lane & 31) * 1024 + h * 16;
    asm volatile("global_load_dwordx4 %0, %1, %2" : "=v"(res) : "v"(vo), "s"(xs) : "memory");
  }
  f32x16 acc0, acc1;
#pragma unroll
  for (int e = 0; e < 16; ++e) { acc0[e] = 0.f; acc1[e] = 0.f; }
  u32x4 af[16];
  auto aload = [&](int i) {
    const int ks = i < 16 ? 16 * kq + i : i < 24 ? 64 + 8 * kq + (i - 16) : i < 28 ? 96 + 4 * kq + (i - 24) : 112 + 8 * kq + (i - 28);
    const char* s = ab0 + ks * 1024;
    asm volatile("global_load_dwordx4 %0, %1, %2 sc1" : "=v"(af[i & 15]) : "v"(l16), "s"(s) : "memory");
  };
  const unsigned fh = WS_FH + (((p2 * 3) * 4 + kq) * 2 + wm2) * 64;
  poll_flags(ws, fh, 8, tag, lane);
#pragma unroll
  for (int i = 0; i < 16; ++i) aload(i);
  poll_flags(ws, fh + 512, 8, tag, lane);
  asm volatile("s_waitcnt vmcnt(0)" : "+v"(res) :: "memory");
  bar();
#pragma unroll
  for (int i = 0; i < 36; ++i) {
    if (i == 12) poll_flags(ws, fh + 1024, 8, tag, lane);
    if (i < 20) asm volatile("s_waitcnt vmcnt(15)" : "+v"(af[i & 15])::"memory");
    else asm volatile("s_waitcnt vmcnt(%1)" : "+v"(af[i & 15]) : "i"(35 - i) : "memory");
    const u32x4 a = af[i & 15];
    u32x4 w0, w1;
    if (i < 18) { w0 = wr[i][0]; w1 = wr[i][1]; }
    else { w0 = tr16(w2a + (i - 18) * 2048, 512); w1 = tr16(w2b + (i - 18) * 2048, 512); }
    acc0 = mfma32(w0, a, acc0);
    acc1 = mfma32(w1, a, acc1);
    __builtin_amdgcn_sched_barrier(0);
    if (i + 16 < 36) aload(i + 16);
  }
  STAMP(6);
  bar();
  float v[8];
#pragma unroll
  for (int d = 0; d < 4; ++d) {
    const f32x16& s = d < 2 ? acc0 : acc1;
    const int o = (d & 1) * 8;
    const int dst = ((rh * 4 + d) * 4 + kq) * 2048 + l16;
    if (d != kq) {
      lds_w128(dst, (u32x4){ubits(s[o]), ubits(s[o + 1]), ubits(s[o + 2]), ubits(s[o + 3])});
      lds_w128(dst + 1024, (u32x4){ubits(s[o + 4]), ubits(s[o + 5]), ubits(s[o + 6]), ubits(s[o + 7])});
    } else {
#pragma unroll
      for (int e = 0; e < 8; ++e) v[e] = s[o + e];
    }
  }
  bar();
#pragma unroll
  for (int n = 1; n < 4; ++n) {
    const int src = ((rh * 4 + kq) * 4 + (kq ^ n)) * 2048 + l16;
    const f32x4 a = __builtin_bit_cast(f32x4, lds_r128(src)), c = __builtin_bit_cast(f32x4, lds_r128(src + 1024));
#pragma unroll
    for (int e = 0; e < 4; ++e) { v[e] += a[e]; v[4 + e] += c[e]; }
  }
#pragma unroll
  for (int e = 0; e < 4; ++e) {
    auto r = __builtin_amdgcn_permlane32_swap(ubits(v[e]), ubits(v[4 + e]), false, false);
    v[e] = fbits(r[0]); v[4 + e] = fbits(r[1]);
  }
  u32x4 o;
#pragma unroll
  for (int e = 0; e < 4; ++e) o[e] = pk2(v[2 * e] + lo16(res[e]), v[2 * e + 1] + hi16(res[e]));
  *(GAS u32x4*)(out + (t64 * 64 + rh * 32 + (lane & 31)) * 1024 + ct64 * 128 + kq * 32 + h * 16) = o;
  if (b == 0 && w < 4) {
    const int id = w * 64 + lane, pp = id >> 4, st = (id >> 3) & 1, aa = id & 7;
    GAS u64* f = (GAS u64*)(ws + WS_FH + ((((pp * 3 + st) * 4 + (aa >> 1)) * 2) * 8 + (aa & 1) * 4) * 8);
  #pragma unroll 1
  for (unsigned spins = 0; spins < SPIN_CAP; ++spins) {
      if (__all(__hip_atomic_load(f, __ATOMIC_RELAXED, __HIP_MEMORY_SCOPE_AGENT) == tag)) break;
      __builtin_amdgcn_s_sleep(2);
    }
  }
  if (b == 0) {
    bar();
    if (threadIdx.x == 0) __hip_atomic_store((GAS u64*)(ws + WS_NONCE), tag, __ATOMIC_RELAXED, __HIP_MEMORY_SCOPE_AGENT);
  }
}

extern "C" void kernel_launch(void* const* d_in, const int* in_sizes, int n_in, void* d_out, int out_size, void* d_ws, size_t ws_size,
                              hipStream_t stream) {
  (void)hipFuncSetAttribute((const void*)fused_block, hipFuncAttributeMaxDynamicSharedMemorySize, LDS_BYTES);
  fused_block<<<256, 512, LDS_BYTES, stream>>>((const char*)d_in[0], (const char*)d_in[1], (const char*)d_in[2], (const char*)d_in[3],
                                               (const char*)d_in[5], (const char*)d_in[6], (const char*)d_in[7], (char*)d_out, (char*)d_ws);
}
```

```cpp
#define STAMPS 1
#define PROBE 101
#define PROBE_BLK 200
#include <hip/hip_runtime.h>
#include <stdint.h>

typedef __attribute__((ext_vector_type(8))) __bf16 b16x8;
typedef __attribute__((ext_vector_type(2))) __bf16 b16x2;
typedef __attribute__((ext_vector_type(4))) short s16x4;
typedef __attribute__((ext_vector_type(4))) float f32x4;
typedef __attribute__((ext_vector_type(2))) float f32x2;
typedef __attribute__((ext_vector_type(16))) float f32x16;
typedef __attribute__((ext_vector_type(4))) unsigned u32x4;
typedef __attribute__((ext_vector_type(2))) unsigned u32x2;
typedef unsigned long long u64;
#define LAS __attribute__((address_space(3)))
#define GAS __attribute__((address_space(1)))
#define DI __device__ __forceinline__

constexpr unsigned WS_NONCE = 0;
constexpr unsigned WS_FKV = 64 << 10;
constexpr unsigned WS_FH = 256 << 10;
constexpr unsigned WS_KVP = 768 << 10;
constexpr unsigned WS_ZP = 1280 << 10;
constexpr unsigned WS_AB = 2u << 20;
constexpr unsigned WS_END = WS_AB + 64u * 144u * 1024u;
constexpr int WSLOT = 20480, NWS = 5, XOFF = 102400, XSLOT = 16384;
constexpr int SCR = 147456, LDS_BYTES = 163840;
constexpr float LOG2E = 1.4426950408889634f;
constexpr unsigned SPIN_CAP = 1u << 15;
constexpr int CODE_LINES = 288;

extern __shared__ __attribute__((aligned(16))) char smem[];

DI unsigned pk2(float a, float b) { f32x2 f = {a, b}; return __builtin_bit_cast(unsigned, __builtin_convertvector(f, b16x2)); }
DI u32x2 pk4(f32x4 v) { return (u32x2){pk2(v[0], v[1]), pk2(v[2], v[3])}; }
DI float lo16(unsigned u) { return __builtin_bit_cast(float, u << 16); }
DI float hi16(unsigned u) { return __builtin_bit_cast(float, u & 0xffff0000u); }
DI float fbits(unsigned u) { return __builtin_bit_cast(float, u); }
DI unsigned ubits(float f) { return __builtin_bit_cast(unsigned, f); }
DI unsigned norm2(unsigned x, float a, float b) {
  const float lo = __builtin_fmaf(lo16(x), a, b);
  float hi = __builtin_fmaf(hi16(x), a, b);
  asm("" : "+v"(hi));
  return pk2(lo, hi);
}
DI f32x4 mfma16(u32x4 a, u32x4 b, f32x4 c) {
  return __builtin_amdgcn_mfma_f32_16x16x32_bf16(__builtin_bit_cast(b16x8, a), __builtin_bit_cast(b16x8, b), c, 0, 0, 0);
}
DI f32x16 mfma32(u32x4 a, u32x4 b, f32x16 c) {
  return __builtin_amdgcn_mfma_f32_32x32x16_bf16(__builtin_bit_cast(b16x8, a), __builtin_bit_cast(b16x8, b), c, 0, 0, 0);
}
DI f32x16 zero16() { f32x16 z; for (int e = 0; e < 16; ++e) z[e] = 0.f; return z; }
DI u32x2 tr8(int off) {
  return __builtin_bit_cast(u32x2, __builtin_amdgcn_ds_read_tr16_b64_v4i16((LAS s16x4*)(smem + off)));
}
DI u32x4 tr16(int off, int second) { u32x2 a = tr8(off), b = tr8(off + second); return (u32x4){a[0], a[1], b[0], b[1]}; }
DI u32x4 lds_r128(int off) { return *(LAS u32x4*)(smem + off); }
DI f32x4 lds_rf(int off) { return *(LAS f32x4*)(smem + off); }
DI void lds_w128(int off, u32x4 v) { *(LAS u32x4*)(smem + off) = v; }
DI void lds_wf(int off, f32x4 v) { *(LAS f32x4*)(smem + off) = v; }
DI void dma_s(unsigned voff, const char* sbase, unsigned lds) {
  asm volatile("s_mov_b32 m0, %2\n\ts_nop 0\n\tglobal_load_lds_dwordx4 %0, %1" ::"v"(voff), "s"(sbase), "s"(lds) : "memory");
}
DI void dma_v(const char* vaddr, unsigned lds) {
  asm volatile("s_mov_b32 m0, %1\n\ts_nop 0\n\tglobal_load_lds_dwordx4 %0, off" ::"v"(vaddr), "s"(lds) : "memory");
}
DI void store_sc1(char* sbase, unsigned voff, u32x4 v) {
  asm volatile("global_store_dwordx4 %0, %1, %2 sc1\n\ts_nop 1" ::"v"(voff), "v"(v), "s"(sbase) : "memory");
}
template <int N> DI void wait_vm() { asm volatile("s_waitcnt vmcnt(%0)" ::"i"(N) : "memory"); }
template <int N> DI void wait_vm_bar() {
  asm volatile("" ::: "memory");
  __builtin_amdgcn_s_waitcnt((N & 15) | (7 << 4) | ((N >> 4) << 14));
  asm volatile("s_barrier" ::: "memory");
}
DI void bar() { wait_vm_bar<63>(); }
#define PIN() __builtin_amdgcn_sched_barrier(0)
template <int CTRL> DI float dpp_add(float v) {
  return v + fbits(__builtin_amdgcn_update_dpp(0u, ubits(v), CTRL, 0xf, 0xf, true));
}
DI float max3(float a, float b, float c, float after) { float r; asm("v_max3_f32 %0, %1, %2, %3" : "=v"(r) : "v"(a), "v"(b), "v"(c), "v"(after)); return r; }
#define KEEP(x) asm volatile("" : "+v"(x))
DI void softmax16(const f32x16& p, u32x4& o0, u32x4& o1) {
  const float m0 = fmaxf(p[0], p[15]);
  const float m = max3(max3(max3(m0, p[1], p[2], m0), max3(p[3], p[4], p[5], m0), max3(p[6], p[7], p[8], m0), m0), max3(p[9], p[10], p[11], m0), max3(p[12], p[13], p[14], m0), m0);
  const float a = -m * LOG2E;
  float e[16];
#pragma unroll
  for (int r = 0; r < 16; ++r) e[r] = __builtin_amdgcn_exp2f(__builtin_fmaf(p[r], LOG2E, a));
  float s[8];
#pragma unroll
  for (int r = 0; r < 8; ++r) s[r] = e[r] + e[r + 8];
  const float inv = __builtin_amdgcn_rcpf(((s[0] + s[4]) + (s[2] + s[6])) + ((s[1] + s[5]) + (s[3] + s[7])));
#pragma unroll
  for (int r = 0; r < 4; ++r) { o0[r] = pk2(e[2 * r] * inv, e[2 * r + 1] * inv); o1[r] = pk2(e[8 + 2 * r] * inv, e[9 + 2 * r] * inv); }
}
DI u32x2 glu4(f32x4 g, f32x4 u) {
  f32x4 h;
#pragma unroll
  for (int r = 0; r < 4; ++r) h[r] = u[r] * __builtin_amdgcn_rcpf(1.0f + __builtin_amdgcn_exp2f(-LOG2E * g[r]));
  return pk4(h);
}
DI u32x4 mk_frag(u32x2 t0, u32x2 t1) {
  auto r0 = __builtin_amdgcn_permlane16_swap(t0[0], t1[0], false, false);
  auto r1 = __builtin_amdgcn_permlane16_swap(t0[1], t1[1], false, false);
  return (u32x4){r0[0], r1[0], r0[1], r1[1]};
}
DI void poll_flags(const char* ws, unsigned off, int n, u64 tag, int lane) {
  GAS u64* f = (GAS u64*)(ws + off + (lane < n ? lane : 0) * 128);
#pragma unroll 1
  for (unsigned spins = 0; spins < SPIN_CAP; ++spins) {
    const u64 v = __hip_atomic_load(f, __ATOMIC_RELAXED, __HIP_MEMORY_SCOPE_AGENT);
    if (__all(v == tag)) break;
    __builtin_amdgcn_s_sleep(2);
  }
  asm volatile("" ::: "memory");
}
DI void set_flag(char* ws, unsigned off, u64 tag, int lane) {
  if (lane == 0) __hip_atomic_store((GAS u64*)(ws + off), tag, __ATOMIC_RELAXED, __HIP_MEMORY_SCOPE_AGENT);
  asm volatile("" ::: "memory");
}
#define STAMP(i) asm volatile("s_memrealtime %0\n\ts_waitcnt lgkmcnt(0)" : "=s"(stamps[i])::"memory")
#define STAMP_ARG , u64 (&stamps)[32]
#define STAMP_PASS , stamps

struct Ptrs { const char *x, *wqkv, *wphi, *wo, *wg, *wu, *wd; char *out, *ws; const char* code; };

DI void w2_piece(const Ptrs& P, int i, int kq, int rh, unsigned voff) {
  int q = kq;
  asm volatile("" : "+s"(q));
  const char* src = i < 16 ? P.wd + (256 * q + 16 * i) * 1024
                  : i < 24 ? P.wd + (1024 + 128 * q + 16 * (i - 16)) * 1024
                  : i < 28 ? P.wd + (1536 + 64 * q + 16 * (i - 24)) * 1024
                           : P.wo + (128 * q + 16 * (i - 28)) * 1024;
  dma_s(voff, src, (i % 18) * 8192 + q * 2048 + rh * 1024);
}
DI unsigned w2_voff(int lane, int rh, int ct64) {
  const int r = lane >> 3;
  return (unsigned)((r + 8 * rh) * 1024 + (((lane & 7) ^ (((r >> 1) & 1) << 2)) * 16) + ct64 * 128);
}
DI void w2_regs(u32x4 (&wr)[18][2], int lane, int kq) {
  const int q4 = (lane & 15) >> 2, blk = (lane >> 4) & 1;
  const int w2r = kq * 2048 + (8 * (lane >> 5) + q4) * 128 + 8 * (lane & 3);
  const int w2a = w2r + ((blk ^ (q4 & 2)) * 32), w2b = w2r + (((2 + blk) ^ (q4 & 2)) * 32);
#pragma unroll
  for (int i = 0; i < 18; ++i) { wr[i][0] = tr16(w2a + i * 8192, 512); wr[i][1] = tr16(w2b + i * 8192, 512); }
}

template <bool TA>
DI void phase1(const Ptrs& P, int lane, int w, int p, int ab, int ct64, u64& tag_out, u32x4 (&wr)[18][2] STAMP_ARG) {
  const int wm = w >> 2, wn = w & 3;
  const int g = lane >> 4, t16 = lane & 15, h = lane >> 5, t32 = lane & 31;
  u32x2 nonce; unsigned wq[8];
  {
    const char* wsn = P.ws;
    const char* wph = P.wphi + (4 * ab + wn) * 512;
    const unsigned z = 0, vo = (unsigned)(h * 256 + ((t32 & 3) + 4 * (t32 >> 3)) * 2);
    asm volatile("global_load_dwordx2 %0, %1, %2 sc1" : "=v"(nonce) : "v"(z), "s"(wsn) : "memory");
    asm volatile("global_load_ushort %0, %8, %9\n\tglobal_load_ushort %1, %8, %9 offset:32\n\t"
                 "global_load_ushort %2, %8, %9 offset:64\n\tglobal_load_ushort %3, %8, %9 offset:96\n\t"
                 "global_load_ushort %4, %8, %9 offset:128\n\tglobal_load_ushort %5, %8, %9 offset:160\n\t"
                 "global_load_ushort %6, %8, %9 offset:192\n\tglobal_load_ushort %7, %8, %9 offset:224"
                 : "=&v"(wq[0]), "=&v"(wq[1]), "=&v"(wq[2]), "=&v"(wq[3]), "=&v"(wq[4]), "=&v"(wq[5]), "=&v"(wq[6]), "=&v"(wq[7])
                 : "v"(vo), "s"(wph) : "memory");
  }
  u32x4 X[16];
  {
    const unsigned voff = (unsigned)((lane >> 3) * 1024 + (lane & 7) * 16);
    const char* xb = P.x + (p * 128 + 16 * w) * 1024;
#pragma unroll
    for (int i = 0; i < 16; ++i) {
      asm volatile("global_load_dwordx4 %0, %1, %2 offset:%3" : "=v"(X[i]) : "v"(voff), "s"(xb + (i & 1) * 8192), "i"((i >> 1) * 128) : "memory");
    }
  }
  {
    const char* cb = P.code;
    const int ln = w * 64 + lane;
    const unsigned vo = (unsigned)(ln < CODE_LINES ? ln : CODE_LINES - 1) * 128;
    asm volatile("s_mov_b32 m0, %2\n\ts_nop 0\n\tglobal_load_lds_dword %0, %1" ::"v"(vo), "s"(cb), "s"(SCR + w * 256) : "memory");
  }
  STAMP(16);
  asm volatile("s_barrier" ::: "memory");
  const int oct = w & 3;
  const int dr = lane >> 3;
  const int dsw = (lane & 7) ^ ((((dr >> 1) & 1) | ((oct & 1) << 1)) << 1);
  const unsigned vq = (unsigned)((8 * oct + dr) * 3072 + dsw * 16), vg = (unsigned)((8 * oct + dr) * 3584 + dsw * 16);
  const char* sp[5];
  const char* vp[2];
  unsigned vo[5]; int pitch[5]; unsigned lofs[5];
  {
    const char* gb[5];
    if (TA) { gb[0] = P.wqkv + (512 + 64 * ab) * 2; gb[1] = P.wqkv + (1024 + 64 * ab) * 2; gb[2] = P.wg + (16 + ab) * 128; gb[3] = P.wu + (16 + ab) * 128; gb[4] = gb[3]; }
    else { gb[0] = P.wqkv + 64 * ab * 2; gb[1] = P.wg + (2 * ab) * 128; gb[2] = P.wu + (2 * ab) * 128; gb[3] = P.wg + (2 * ab + 1) * 128; gb[4] = P.wu + (2 * ab + 1) * 128; }
#pragma unroll
    for (int m = 0; m < 5; ++m) {
      const int n0 = 8 * m, n1 = 8 * m + 4;
      const int G0 = (n0 % 20) / 4, G1 = (n1 % 20) / 4, S0 = n0 / 20, S1 = n1 / 20;
      const bool q0 = G0 == 0 || (TA && G0 == 1), q1 = G1 == 0 || (TA && G1 == 1);
      pitch[m] = wm ? (q1 ? 3072 : 3584) : (q0 ? 3072 : 3584);
      sp[m] = (wm ? gb[G1] : gb[G0]) + (wm ? S1 : S0) * 32 * pitch[m];
      vo[m] = wm ? (q1 ? vq : vg) : (q0 ? vq : vg);
      lofs[m] = (wm ? G1 : G0) * 4096 + oct * 1024;
    }
    if (TA) {
      const char* hp = (dsw < 4 ? P.wg : P.wu) + (24 + (ab >> 1)) * 128 + (ab & 1) * 64 + (8 * oct + dr) * 3584 + (dsw & 3) * 16;
      vp[0] = wm ? sp[2] + vo[2] : hp;
      vp[1] = wm ? hp + 32 * 3584 : sp[4] + vo[4];
    }
  }
  auto wpiece = [&](int D, int m) {
    const unsigned sa = ((2 * D + 4) % NWS) * WSLOT, sb = ((2 * D + 5) % NWS) * WSLOT;
    unsigned lb = lofs[m], hw = wm;
    asm volatile("" : "+s"(lb), "+s"(hw));
    const unsigned lds = lb + (m < 2 ? sa : m > 2 ? sb : sa + hw * (sb - sa));
    if (TA && (m == 2 || m == 4)) { dma_v(vp[m == 4], lds); vp[m == 4] += 64 * pitch[m]; }
    else { dma_s(vo[m], sp[m], lds); sp[m] += 64 * pitch[m]; }
  };
  float s1[2] = {0.f, 0.f}, s2[2] = {0.f, 0.f};
  const b16x2 ones2 = __builtin_bit_cast(b16x2, 0x3f803f80u);
#define XWAIT(N, i) asm volatile("s_waitcnt vmcnt(%4)" : "+v"(X[i]), "+v"(X[i + 1]), "+v"(X[i + 2]), "+v"(X[i + 3]) : "i"(N) : "memory")
#pragma unroll
  for (int q = 0; q < 4; ++q) {
#pragma unroll
    for (int n = 3 * q; n < 3 * q + 3; ++n) wpiece(n / 5, n % 5);
    if (q == 0) { XWAIT(16, 0); asm volatile("" : "+v"(nonce), "+v"(wq[0]), "+v"(wq[1]), "+v"(wq[2]), "+v"(wq[3]), "+v"(wq[4]), "+v"(wq[5]), "+v"(wq[6]), "+v"(wq[7])); }
    if (q == 1) XWAIT(15, 4);
    if (q == 2) XWAIT(14, 8);
    if (q == 3) XWAIT(13, 12);
    STAMP(17 + q);
#pragma unroll
    for (int i = 4 * q; i < 4 * q + 4; ++i)
#pragma unroll
      for (int e = 0; e < 4; ++e) {
        const unsigned xv = X[i][e];
        const b16x2 v = __builtin_bit_cast(b16x2, xv);
        s1[i & 1] = __builtin_amdgcn_fdot2_f32_bf16(v, ones2, s1[i & 1], false);
        s2[i & 1] = __builtin_amdgcn_fdot2_f32_bf16(v, v, s2[i & 1], false);
      }
    PIN();
  }
  STAMP(0);
  const u64 nn = ((u64)(unsigned)__builtin_amdgcn_readfirstlane(nonce[1]) << 32) | (unsigned)__builtin_amdgcn_readfirstlane(nonce[0]);
  const u64 tag = nn * 6364136223846793005ull + 1442695040888963407ull;
  tag_out = tag;
  u32x4 wphi = {wq[0] | (wq[1] << 16), wq[2] | (wq[3] << 16), wq[4] | (wq[5] << 16), wq[6] | (wq[7] << 16)};
  wphi ^= (t32 & 4) ? 0x80008000u : 0u;
  asm volatile("" : "+v"(wphi));
  float na[2], nb[2];
#pragma unroll
  for (int r = 0; r < 2; ++r) {
    float a = s1[r], b = s2[r];
    a = dpp_add<0xB1>(a); b = dpp_add<0xB1>(b);
    a = dpp_add<0x4E>(a); b = dpp_add<0x4E>(b);
    a = dpp_add<0x141>(a); b = dpp_add<0x141>(b);
    const float m = a * (1.0f / 512.0f);
    const float var = fmaxf(b * (1.0f / 512.0f) - m * m, 0.0f);
    na[r] = __builtin_amdgcn_rsqf(var + 1e-5f);
    nb[r] = -m * na[r];
  }
  const int xw = XOFF + (16 * w + dr) * 128 + (((lane & 7) ^ ((dr >> 1) & 3)) * 16);
  auto xaddr = [&](int i) { return ((xw + (i & 1) * 1024) ^ ((i & 1) * 64)) + (((i >> 1) + 1) & 1) * XSLOT; };
  auto xwrite = [&](int i) {
    u32x4 o;
#pragma unroll
    for (int e = 0; e < 4; ++e) o[e] = norm2(X[i][e], na[i & 1], nb[i & 1]);
    lds_w128(xaddr(i), o);
  };
  const int q4 = t16 >> 2, p4 = lane & 3;
  const int fsw = ((q4 >> 1) & 1) | ((g & 1) << 1);
  const int trb = g * 1024 + q4 * 128 + 8 * p4;
  const int tr0 = trb + ((wn ^ fsw) * 32);
  const int trg = trb + (((wn >> 1) ^ fsw) * 32) + 4 * 4096, tru = trb + (((2 + (wn >> 1)) ^ fsw) * 32) + 4 * 4096;
  const int xr = XOFF + (64 * wm + t16) * 128 + ((g ^ (t16 >> 1)) * 16);
  const int pr = TA ? (wn & 1) : 0;
  const int xrA = xr + pr * 4096, xrB = xr + (1 - pr) * 4096;
  const unsigned v2 = w2_voff(lane, wm, ct64);
  constexpr int NWF = TA ? 6 : 5, NRD = 2 * NWF + 4;
  u32x2 wf[2][NWF][2];
  u32x4 xf[2][4];
  auto fread = [&](int s, int r) {
    const int slot = ((s + 4) % NWS) * WSLOT, b = s & 1, xs = (((s >> 1) + 1) & 1) * XSLOT;
    if (r < 4) xf[b][r] = lds_r128(((r < 2 ? xrA : xrB) ^ (b * 64)) + xs + (r & 1) * 2048);
    else {
      const int G = (r - 4) >> 1, t = (r - 4) & 1;
      wf[b][G][t] = tr8(((TA && G == 4) ? trg : (TA && G == 5) ? tru : tr0 + G * 4096) + slot + t * 512);
    }
  };
  f32x4 acc[5][4];
#pragma unroll
  for (int a = 0; a < 5; ++a)
#pragma unroll
    for (int b = 0; b < 4; ++b) acc[a][b] = (f32x4){0.f, 0.f, 0.f, 0.f};
  xwrite(0); xwrite(1);
  wait_vm_bar<9>();
  STAMP(1);
#pragma unroll
  for (int r = 0; r < NRD; ++r) fread(0, r);
  xwrite(2);
#pragma unroll
  for (int s = 0; s < 16; ++s) {
    if (s <= 10) wait_vm_bar<7>(); else if (s <= 13) wait_vm_bar<8>(); else if (s == 14) wait_vm_bar<10>();
    PIN();
    const int b = s & 1;
    u32x4 xo;
#pragma unroll
    for (int k = 0; k < 20; ++k) {
      const int G = k >> 2, rt = k & 3;
      if (TA && G == 4) {
        const int gi = 4 + (rt >> 1);
        acc[4][rt] = mfma16((u32x4){wf[b][gi][0][0], wf[b][gi][0][1], wf[b][gi][1][0], wf[b][gi][1][1]}, xf[b][rt & 1], acc[4][rt]);
      } else {
        const u32x4 wv = {wf[b][G][0][0], wf[b][G][0][1], wf[b][G][1][0], wf[b][G][1][1]};
        acc[G][rt] = (TA && G == 1) ? mfma16(xf[b][rt], wv, acc[G][rt]) : mfma16(wv, xf[b][rt], acc[G][rt]);
      }
      asm volatile("" : "+v"(acc[G][rt]));
      PIN();
      if (s <= 10) {
        if (s & 1) { if (k == 0) wpiece((s + 5) / 2, 0); if (k == 6) wpiece((s + 5) / 2, 1); }
        else { if (k == 0) wpiece((s + 4) / 2, 2); if (k == 5) wpiece((s + 4) / 2, 3); if (k == 10) wpiece((s + 4) / 2, 4); }
      }
      if (s == 10 && k == 15) w2_piece(P, 17, wn, wm, v2);
      if (s == 11) { if (k == 0) w2_piece(P, 0, wn, wm, v2); if (k == 6) w2_piece(P, 1, wn, wm, v2); }
      if (s == 12) { if (k == 0) w2_piece(P, 2, wn, wm, v2); if (k == 5) w2_piece(P, 3, wn, wm, v2); if (k == 10) w2_piece(P, 4, wn, wm, v2); }
      if (s == 13) { if (k == 0) w2_piece(P, 5, wn, wm, v2); if (k == 5) w2_piece(P, 6, wn, wm, v2); if (k == 10) w2_piece(P, 15, wn, wm, v2); if (k == 15) w2_piece(P, 16, wn, wm, v2); }
      if (s == 14) { if (k == 0) w2_piece(P, 7, wn, wm, v2); if (k == 5) w2_piece(P, 8, wn, wm, v2); if (k == 10) w2_piece(P, 9, wn, wm, v2); }
      if (s < 15 && k >= 1 && k - 1 < NRD) fread(s + 1, k - 1);
      if (s + 3 < 16) {
        const int i = s + 3;
        if ((k & 3) == 3 && k < 16) xo[k >> 2] = norm2(X[i][k >> 2], na[i & 1], nb[i & 1]);
        if (k == 16) lds_w128(xaddr(i), xo);
      }
      if (s == 15 && k == 0) {
        bar();
#pragma unroll
        for (int i = 10; i < 15; ++i) w2_piece(P, i, wn, wm, v2);
      }
      PIN();
    }
  }
  STAMP(2);
  const unsigned abase = WS_AB + (unsigned)(p * 4 + wm * 2) * (144 * 1024);
  const unsigned l16 = lane * 16;
  const int scr = SCR + w * 2048, scr_o = SCR + (w ^ 4) * 2048;
  const unsigned fh = WS_FH + ((((p * 2) * 4 + (ab >> 1)) * 2 + wm) * 16 + (ab & 1) * 4 + wn) * 128;
  if (TA) {
    f32x16 kv = zero16();
    const int iw = scr + t32 * 64, sw = (t32 >> 1) & 3;
    const int rr = 8 * h + q4, uu = (p4 & 1) * 4 + 2 * (g & 1) + (p4 >> 1);
    const int ir0 = scr + rr * 64 + (((uu >> 1) ^ ((rr >> 1) & 3)) * 16) + (uu & 1) * 8;
    const int ir1 = scr + (rr + 4) * 64 + (((uu >> 1) ^ (((rr + 4) >> 1) & 3)) * 16) + (uu & 1) * 8;
#pragma unroll
    for (int pp = 0; pp < 2; ++pp) {
      u32x4 f0, f1;
      softmax16(mfma32(wphi, mk_frag(pk4(acc[0][2 * pp]), pk4(acc[0][2 * pp + 1])), zero16()), f0, f1);
      lds_w128(iw + (((2 * h) ^ sw) * 16), f0);
      lds_w128(iw + (((2 * h + 1) ^ sw) * 16), f1);
#pragma unroll
      for (int e = 0; e < 2; ++e) {
        const u32x2 a0 = tr8(ir0 + e * 1024), a1 = tr8(ir1 + e * 1024);
        const u32x4 vb = mk_frag(pk4(acc[1][2 * pp + e]), (u32x2){0x3f803f80u, 0x3f803f80u});
        kv = mfma32((u32x4){a0[0], a0[1], a1[0], a1[1]}, vb, kv);
      }
    }
    STAMP(11);
    f32x4 k0, k1;
    if (wm == 0) {
      lds_wf(scr + l16, (f32x4){kv[8], kv[9], kv[10], kv[11]}); lds_wf(scr + 1024 + l16, (f32x4){kv[12], kv[13], kv[14], kv[15]});
      k0 = (f32x4){kv[0], kv[1], kv[2], kv[3]}; k1 = (f32x4){kv[4], kv[5], kv[6], kv[7]};
    } else {
      lds_wf(scr + l16, (f32x4){kv[0], kv[1], kv[2], kv[3]}); lds_wf(scr + 1024 + l16, (f32x4){kv[4], kv[5], kv[6], kv[7]});
      k0 = (f32x4){kv[8], kv[9], kv[10], kv[11]}; k1 = (f32x4){kv[12], kv[13], kv[14], kv[15]};
    }
    bar();
    k0 += lds_rf(scr_o + l16); k1 += lds_rf(scr_o + 1024 + l16);
    {
      const u32x2 a = pk4(k0), b = pk4(k1);
      const unsigned id = (ab * 4 + wn) * 16 + p;
      const unsigned off = t32 < 16 ? WS_KVP + id * 1024 + wm * 512 + h * 256 + t16 * 16 : WS_ZP + id * 64 + h * 32 + wm * 16;
      if (t32 <= 16) store_sc1(P.ws, off, (u32x4){a[0], a[1], b[0], b[1]});
    }
    asm volatile("" ::: "memory");
    const u32x4 fr0 = mk_frag(glu4(acc[2][0], acc[3][0]), glu4(acc[2][1], acc[3][1]));
    const u32x4 fr1 = mk_frag(glu4(acc[2][2], acc[3][2]), glu4(acc[2][3], acc[3][3]));
    store_sc1(P.ws + (abase + pr * (144 * 1024) + (64 + 4 * ab + wn) * 1024), l16, fr0);
    store_sc1(P.ws + (abase + (1 - pr) * (144 * 1024) + (64 + 4 * ab + wn) * 1024), l16, fr1);
    wait_vm<2>();
    set_flag(P.ws, WS_FKV + (((ab * 4 + wn) * 16 + p) * 2 + wm) * 128, tag, lane);
    STAMP(3);
    const u32x4 fr2 = mk_frag(glu4(acc[4][0], acc[4][2]), glu4(acc[4][1], acc[4][3]));
    store_sc1(P.ws + (abase + pr * (144 * 1024) + (96 + 2 * ab + (wn >> 1)) * 1024), l16, fr2);
    bar();
    w2_regs(wr, lane, wn);
    wait_vm<0>();
    set_flag(P.ws, fh + 8 * 128, tag, lane);
    STAMP(4);
    bar();
#pragma unroll
    for (int i = 18; i < 27; ++i) w2_piece(P, i, wn, wm, v2);
  } else {
#pragma unroll
    for (int h2 = 0; h2 < 2; ++h2)
#pragma unroll
      for (int pp = 0; pp < 2; ++pp) {
        const u32x4 fr4 = mk_frag(glu4(acc[1 + 2 * h2][2 * pp], acc[2 + 2 * h2][2 * pp]), glu4(acc[1 + 2 * h2][2 * pp + 1], acc[2 + 2 * h2][2 * pp + 1]));
        store_sc1(P.ws + (abase + pp * (144 * 1024) + (8 * ab + 4 * h2 + wn) * 1024), l16, fr4);
      }
    STAMP(11);
    wait_vm_bar<4>();
    w2_regs(wr, lane, wn);
    u32x4 ph[2][2];
#pragma unroll
    for (int pp = 0; pp < 2; ++pp)
      softmax16(mfma32(wphi, mk_frag(pk4(acc[0][2 * pp]), pk4(acc[0][2 * pp + 1])), zero16()), ph[pp][0], ph[pp][1]);
    KEEP(ph[0][0]); KEEP(ph[0][1]); KEEP(ph[1][0]); KEEP(ph[1][1]);
    wait_vm<0>();
    set_flag(P.ws, fh, tag, lane);
    STAMP(3);
    bar();
    poll_flags(P.ws, WS_FKV + (ab * 4 + wn) * 32 * 128, 32, tag, lane);
    STAMP(12);
    u32x4 part[8], zq;
    {
      const char* kb = P.ws + WS_KVP + ((ab * 4 + wn) * 16 + 8 * wm) * 1024;
      const char* zb = P.ws + WS_ZP + (ab * 4 + wn) * 1024;
      const unsigned vk = (g & 1) * 4096 + h * 256 + t16 * 16, vz = t16 * 64 + g * 16;
      asm volatile("global_load_dwordx4 %0, %1, %2 sc1" : "=v"(zq) : "v"(vz), "s"(zb) : "memory");
#pragma unroll
      for (int i = 0; i < 8; ++i) asm volatile("global_load_dwordx4 %0, %1, %2 offset:%3 sc1" : "=v"(part[i]) : "v"(vk), "s"(kb), "i"(i * 512) : "memory");
    }
#pragma unroll
    for (int i = 18; i < 27; ++i) w2_piece(P, i, wn, wm, v2);
    STAMP(21);
    asm volatile("s_waitcnt vmcnt(17)" : "+v"(zq)::"memory");
    STAMP(22);
    float z[8];
#pragma unroll
    for (int e = 0; e < 4; ++e) { z[2 * e] = lo16(zq[e]); z[2 * e + 1] = hi16(zq[e]); }
#pragma unroll
    for (int e = 0; e < 8; ++e) { z[e] = dpp_add<0xB1>(z[e]); z[e] = dpp_add<0x4E>(z[e]); z[e] = dpp_add<0x141>(z[e]); z[e] = dpp_add<0x140>(z[e]); }
    u32x4 zp = {pk2(z[0], z[1]), pk2(z[2], z[3]), pk2(z[4], z[5]), pk2(z[6], z[7])};
    KEEP(zp);
    asm volatile("s_waitcnt vmcnt(9)" : "+v"(part[0]), "+v"(part[1]), "+v"(part[2]), "+v"(part[3]), "+v"(part[4]), "+v"(part[5]), "+v"(part[6]), "+v"(part[7])::"memory");
    STAMP(23);
    f32x4 k0, k1;
#pragma unroll
    for (int e = 0; e < 8; ++e) {
      float a = 0.f, b = 0.f;
#pragma unroll
      for (int i = 0; i < 4; ++i) {
        const unsigned ua = part[2 * i][e >> 1], ub = part[2 * i + 1][e >> 1];
        a += (e & 1) ? hi16(ua) : lo16(ua); b += (e & 1) ? hi16(ub) : lo16(ub);
      }
      auto r = __builtin_amdgcn_permlane16_swap(ubits(a), ubits(b), false, false);
      const float v = fbits(r[0]) + fbits(r[1]);
      if (e < 4) k0[e] = v; else k1[e - 4] = v;
    }
    lds_wf(scr + l16, k0); lds_wf(scr + 1024 + l16, k1);
    bar();
    k0 += lds_rf(scr_o + l16); k1 += lds_rf(scr_o + 1024 + l16);
    u32x4 a0, a1;
    {
      const u32x2 ka = pk4(k0), kb = pk4(k1);
      const unsigned kp[4] = {ka[0], ka[1], kb[0], kb[1]};
#pragma unroll
      for (int e = 0; e < 4; ++e) {
        auto r = __builtin_amdgcn_permlane16_swap(kp[e], zp[e], false, false);
        a0[e] = r[0]; a1[e] = r[1];
      }
    }
#pragma unroll
    for (int pp = 0; pp < 2; ++pp) {
      const f32x16 nd = mfma32(a1, ph[pp][1], mfma32(a0, ph[pp][0], zero16()));
      const float r = __builtin_amdgcn_rcpf(nd[8] + 1e-6f);
      auto s0 = __builtin_amdgcn_permlane32_swap(pk2(nd[0] * r, nd[1] * r), pk2(nd[4] * r, nd[5] * r), false, false);
      auto s1 = __builtin_amdgcn_permlane32_swap(pk2(nd[2] * r, nd[3] * r), pk2(nd[6] * r, nd[7] * r), false, false);
      store_sc1(P.ws + (abase + pp * (144 * 1024) + (112 + 4 * ab + wn) * 1024), l16, (u32x4){s0[0], s1[0], s0[1], s1[1]});
    }
    STAMP(24);
    wait_vm<0>();
    set_flag(P.ws, fh + 4 * 2 * 16 * 128, tag, lane);
    STAMP(4);
  }
}

__global__ __launch_bounds__(512, 2) void fused_block(const char* x, const char* wqkv, const char* wphi, const char* wo, const char* wg,
                                                      const char* wu, const char* wd, char* out, char* ws) {
  u64 pc; asm volatile("s_getpc_b64 %0" : "=s"(pc));
  const int lane = threadIdx.x & 63;
  int w = __builtin_amdgcn_readfirstlane(threadIdx.x >> 6);
  const int b = blockIdx.x, xcd = b & 7, bi = b >> 3;
  const Ptrs P = {x, wqkv, wphi, wo, wg, wu, wd, out, ws, (const char*)(pc & ~(u64)0xff)};
  const int p = (xcd & 1) * 8 + (bi & 7), jj = bi >> 3, ab = (xcd >> 1) * 2 + (jj & 1);
  int t64 = xcd * 4 + (bi >> 3), ct64 = bi & 7;
  u64 stamps[32];
#pragma unroll
  for (int i = 0; i < 32; ++i) stamps[i] = 0;
  asm volatile("s_memtime %0\n\ts_waitcnt lgkmcnt(0)" : "=s"(stamps[9])::"memory");
  STAMP(7);
  if (PROBE > 100 && threadIdx.x == 0) __hip_atomic_store((GAS u64*)(ws + WS_END + b * 8), stamps[7], __ATOMIC_RELAXED, __HIP_MEMORY_SCOPE_AGENT);
  u64 tag;
  u32x4 wr[18][2];
  if (jj < 2) phase1<true>(P, lane, w, p, ab, ct64, tag, wr STAMP_PASS);
  else phase1<false>(P, lane, w, p, ab, ct64, tag, wr STAMP_PASS);

  asm volatile("" : "+s"(w), "+s"(t64), "+s"(ct64));
  const int kq = w & 3, rh = w >> 2;
  const int h = lane >> 5, q4 = (lane & 15) >> 2, blk = (lane >> 4) & 1;
  const int w2r = kq * 2048 + (8 * h + q4) * 128 + 8 * (lane & 3);
  const int w2a = w2r + ((blk ^ (q4 & 2)) * 32), w2b = w2r + (((2 + blk) ^ (q4 & 2)) * 32);
  const unsigned v2 = w2_voff(lane, rh, ct64);
  const char* ab0 = ws + WS_AB + (unsigned)(t64 * 2 + rh) * (144 * 1024);
  const unsigned l16 = lane * 16;
  STAMP(5);
  u32x4 res;
  {
    const char* xs = x + (t64 * 64 + rh * 32) * 1024 + ct64 * 128 + kq * 32;
    const unsigned vo = (lane & 31) * 1024 + h * 16;
    asm volatile("global_load_dwordx4 %0, %1, %2" : "=v"(res) : "v"(vo), "s"(xs) : "memory");
  }
  f32x16 acc0 = zero16(), acc1 = zero16();
  u32x4 af[16];
  auto aload = [&](int i) {
    int q = kq;
    asm volatile("" : "+s"(q));
    const int i4 = i & ~3;
    const int ks = i4 < 16 ? 16 * q + i4 : i4 < 24 ? 64 + 8 * q + (i4 - 16) : i4 < 28 ? 96 + 4 * q : 112 + 8 * q + (i4 - 28);
    const char* s = ab0 + ks * 1024;
    asm volatile("global_load_dwordx4 %0, %1, %2 offset:%3 sc1" : "=v"(af[i & 15]) : "v"(l16), "s"(s), "i"((i & 3) * 1024) : "memory");
  };
  const unsigned fh = WS_FH + ((((t64 >> 1) * 2) * 4 + kq) * 2 + (t64 & 1)) * 16 * 128;
  poll_flags(ws, fh, 16, tag, lane);
  asm volatile("" : "+v"(res));
  STAMP(13);
#pragma unroll
  for (int i = 0; i < 16; ++i) aload(i);
#pragma unroll
  for (int i = 27; i < 36; ++i) w2_piece(P, i, kq, rh, v2);
  u32x2 fl;
  u32x4 wn0, wn1;
#pragma unroll
  for (int i = 0; i < 36; ++i) {
    if (i == 1) STAMP(25);
    if (i == 12) {
      STAMP(26);
      asm volatile("s_waitcnt vmcnt(6)" : "+v"(fl)::"memory");
      const u64 v = ((u64)fl[1] << 32) | fl[0];
      if (!__all(v == tag)) poll_flags(ws, fh + 4 * 2 * 16 * 128, 8, tag, lane);
    }
    if (i == 12) STAMP(27);
    if (i == 28) STAMP(28);
    if (i == 17 || i == 30) bar();
    if (i < 16) asm volatile("s_waitcnt vmcnt(24)" : "+v"(af[i & 15])::"memory");
    else if (i < 20) asm volatile("s_waitcnt vmcnt(15)" : "+v"(af[i & 15])::"memory");
    else asm volatile("s_waitcnt vmcnt(%1)" : "+v"(af[i & 15]) : "i"(35 - i) : "memory");
    const u32x4 a = af[i & 15];
    const u32x4 w0 = i < 18 ? wr[i < 18 ? i : 0][0] : wn0, w1 = i < 18 ? wr[i < 18 ? i : 0][1] : wn1;
    acc0 = mfma32(w0, a, acc0);
    asm volatile("" : "+v"(acc0));
    if (i >= 17 && i < 35) wn0 = tr16(w2a + (i - 17) * 8192, 512);
    acc1 = mfma32(w1, a, acc1);
    asm volatile("" : "+v"(acc1));
    if (i >= 17 && i < 35) wn1 = tr16(w2b + (i - 17) * 8192, 512);
    PIN();
    if (i + 16 < 36) aload(i + 16);
    if (i == 5) {
      const char* fb = ws + fh + 4 * 2 * 16 * 128;
      const unsigned vo = (lane & 7) * 128;
      asm volatile("global_load_dwordx2 %0, %1, %2 sc1" : "=v"(fl) : "v"(vo), "s"(fb) : "memory");
    }
    PIN();
  }
  STAMP(6);
  float v[8];
#pragma unroll
  for (int d = 0; d < 4; ++d) {
    const f32x16& s = d < 2 ? acc0 : acc1;
    const int o = (d & 1) * 8;
    const int dst = ((rh * 4 + d) * 4 + kq) * 2048 + l16;
    if (d != kq) {
      lds_wf(dst, (f32x4){s[o], s[o + 1], s[o + 2], s[o + 3]});
      lds_wf(dst + 1024, (f32x4){s[o + 4], s[o + 5], s[o + 6], s[o + 7]});
    } else {
#pragma unroll
      for (int e = 0; e < 8; ++e) v[e] = s[o + e];
    }
  }
  bar();
#pragma unroll
  for (int n = 1; n < 4; ++n) {
    const int src = ((rh * 4 + kq) * 4 + (kq ^ n)) * 2048 + l16;
    const f32x4 a = lds_rf(src), c = lds_rf(src + 1024);
#pragma unroll
    for (int e = 0; e < 4; ++e) { v[e] += a[e]; v[4 + e] += c[e]; }
  }
#pragma unroll
  for (int e = 0; e < 4; ++e) {
    auto r = __builtin_amdgcn_permlane32_swap(ubits(v[e]), ubits(v[4 + e]), false, false);
    v[e] = fbits(r[0]); v[4 + e] = fbits(r[1]);
  }
  u32x4 o;
#pragma unroll
  for (int e = 0; e < 4; ++e) o[e] = pk2(v[2 * e] + lo16(res[e]), v[2 * e + 1] + hi16(res[e]));
  *(GAS u32x4*)(out + (t64 * 64 + rh * 32 + (lane & 31)) * 1024 + ct64 * 128 + kq * 32 + h * 16) = o;
  if (b == 0) {
    if (w < 4) {
      const int id = w * 64 + lane, pp = id >> 4, ty = (id >> 3) & 1, aa = id & 7;
      GAS u64* f = (GAS u64*)(ws + WS_FH + ((((pp * 2) * 4 + (aa >> 1)) * 2) * 16 + ty * 8 + (aa & 1) * 4) * 128);
#pragma unroll 1
      for (unsigned spins = 0; spins < SPIN_CAP; ++spins) {
        if (__all(__hip_atomic_load(f, __ATOMIC_RELAXED, __HIP_MEMORY_SCOPE_AGENT) == tag)) break;
        __builtin_amdgcn_s_sleep(2);
      }
    }
    bar();
    if (threadIdx.x == 0) __hip_atomic_store((GAS u64*)(ws + WS_NONCE), tag, __ATOMIC_RELAXED, __HIP_MEMORY_SCOPE_AGENT);
  }
  STAMP(8);
  asm volatile("s_memtime %0\n\ts_waitcnt lgkmcnt(0)" : "=s"(stamps[10])::"memory");
  if (b == PROBE_BLK && w == 0) {
    u64 d = PROBE == 100 ? (stamps[10] - stamps[9]) / 8 : 4 * (stamps[PROBE % 32] - stamps[7]);
    if (PROBE > 100) {
      u64 mn = ~0ull, mx = 0;
      for (int i = 0; i < 256; ++i) { const u64 e = __hip_atomic_load((GAS u64*)(ws + WS_END + i * 8), __ATOMIC_RELAXED, __HIP_MEMORY_SCOPE_AGENT); mn = e < mn ? e : mn; mx = e > mx ? e : mx; }
      d = PROBE == 101 ? 0 : PROBE == 102 ? 16 * (stamps[7] - mn) : 16 * (mx - mn);
      STAMP(8);
    }
    const u64 until = stamps[8] + (d < 100000 ? d : 100000);
    while (__builtin_amdgcn_s_memrealtime() < until) __builtin_amdgcn_s_sleep(1);
  }
}

extern "C" void kernel_launch(void* const* d_in, const int* in_sizes, int n_in, void* d_out, int out_size, void* d_ws, size_t ws_size,
                              hipStream_t stream) {
  (void)hipFuncSetAttribute((const void*)fused_block, hipFuncAttributeMaxDynamicSharedMemorySize, LDS_BYTES);
  fused_block<<<256, 512, LDS_BYTES, stream>>>((const char*)d_in[0], (const char*)d_in[1], (const char*)d_in[2], (const char*)d_in[3],
                                               (const char*)d_in[5], (const char*)d_in[6], (const char*)d_in[7], (char*)d_out, (char*)d_ws);
}
```

```cpp
#define STAMPS 1
#define GAPS 0
#include <hip/hip_runtime.h>
#include <stdint.h>

typedef __attribute__((ext_vector_type(8))) __bf16 b16x8;
typedef __attribute__((ext_vector_type(2))) __bf16 b16x2;
typedef __attribute__((ext_vector_type(4))) short s16x4;
typedef __attribute__((ext_vector_type(4))) float f32x4;
typedef __attribute__((ext_vector_type(2))) float f32x2;
typedef __attribute__((ext_vector_type(16))) float f32x16;
typedef __attribute__((ext_vector_type(4))) unsigned u32x4;
typedef __attribute__((ext_vector_type(2))) unsigned u32x2;
typedef unsigned long long u64;
#define LAS __attribute__((address_space(3)))
#define GAS __attribute__((address_space(1)))
#define DI __device__ __forceinline__

constexpr unsigned WS_NONCE = 0;
constexpr unsigned WS_FKV = 64 << 10;
constexpr unsigned WS_FH = 256 << 10;
constexpr unsigned WS_KVP = 768 << 10;
constexpr unsigned WS_ZP = 1280 << 10;
constexpr unsigned WS_AB = 2u << 20;
constexpr unsigned WS_END = WS_AB + 64u * 144u * 1024u;
constexpr int WSLOT = 20480, NWS = 5, XOFF = 102400, XSLOT = 16384;
constexpr int SCR = 147456, LDS_BYTES = 163840;
constexpr float LOG2E = 1.4426950408889634f;
constexpr unsigned SPIN_CAP = 1u << 15;
constexpr int CODE_LINES = 288;

extern __shared__ __attribute__((aligned(16))) char smem[];

DI unsigned pk2(float a, float b) { f32x2 f = {a, b}; return __builtin_bit_cast(unsigned, __builtin_convertvector(f, b16x2)); }
DI u32x2 pk4(f32x4 v) { return (u32x2){pk2(v[0], v[1]), pk2(v[2], v[3])}; }
DI float lo16(unsigned u) { return __builtin_bit_cast(float, u << 16); }
DI float hi16(unsigned u) { return __builtin_bit_cast(float, u & 0xffff0000u); }
DI float fbits(unsigned u) { return __builtin_bit_cast(float, u); }
DI unsigned ubits(float f) { return __builtin_bit_cast(unsigned, f); }
DI unsigned norm2(unsigned x, float a, float b) {
  const float lo = __builtin_fmaf(lo16(x), a, b);
  float hi = __builtin_fmaf(hi16(x), a, b);
  asm("" : "+v"(hi));
  return pk2(lo, hi);
}
DI f32x4 mfma16(u32x4 a, u32x4 b, f32x4 c) {
  return __builtin_amdgcn_mfma_f32_16x16x32_bf16(__builtin_bit_cast(b16x8, a), __builtin_bit_cast(b16x8, b), c, 0, 0, 0);
}
DI f32x16 mfma32(u32x4 a, u32x4 b, f32x16 c) {
  return __builtin_amdgcn_mfma_f32_32x32x16_bf16(__builtin_bit_cast(b16x8, a), __builtin_bit_cast(b16x8, b), c, 0, 0, 0);
}
DI f32x16 zero16() { f32x16 z; for (int e = 0; e < 16; ++e) z[e] = 0.f; return z; }
DI u32x2 tr8(int off) {
  return __builtin_bit_cast(u32x2, __builtin_amdgcn_ds_read_tr16_b64_v4i16((LAS s16x4*)(smem + off)));
}
DI u32x4 tr16(int off, int second) { u32x2 a = tr8(off), b = tr8(off + second); return (u32x4){a[0], a[1], b[0], b[1]}; }
DI u32x4 lds_r128(int off) { return *(LAS u32x4*)(smem + off); }
DI f32x4 lds_rf(int off) { return *(LAS f32x4*)(smem + off); }
DI void lds_w128(int off, u32x4 v) { *(LAS u32x4*)(smem + off) = v; }
DI void lds_wf(int off, f32x4 v) { *(LAS f32x4*)(smem + off) = v; }
DI void dma_s(unsigned voff, const char* sbase, unsigned lds) {
  asm volatile("s_mov_b32 m0, %2\n\ts_nop 0\n\tglobal_load_lds_dwordx4 %0, %1" ::"v"(voff), "s"(sbase), "s"(lds) : "memory");
}
DI void dma_v(const char* vaddr, unsigned lds) {
  asm volatile("s_mov_b32 m0, %1\n\ts_nop 0\n\tglobal_load_lds_dwordx4 %0, off" ::"v"(vaddr), "s"(lds) : "memory");
}
DI void store_sc1(char* sbase, unsigned voff, u32x4 v) {
  asm volatile("global_store_dwordx4 %0, %1, %2 sc1\n\ts_nop 1" ::"v"(voff), "v"(v), "s"(sbase) : "memory");
}
template <int N> DI void wait_vm() { asm volatile("s_waitcnt vmcnt(%0)" ::"i"(N) : "memory"); }
template <int N> DI void wait_vm_bar() {
  asm volatile("" ::: "memory");
  __builtin_amdgcn_s_waitcnt((N & 15) | (7 << 4) | ((N >> 4) << 14));
  asm volatile("s_barrier" ::: "memory");
}
DI void bar() { wait_vm_bar<63>(); }
#define PIN() __builtin_amdgcn_sched_barrier(0)
template <int CTRL> DI float dpp_add(float v) {
  return v + fbits(__builtin_amdgcn_update_dpp(0u, ubits(v), CTRL, 0xf, 0xf, true));
}
DI float max3(float a, float b, float c, float after) { float r; asm("v_max3_f32 %0, %1, %2, %3" : "=v"(r) : "v"(a), "v"(b), "v"(c), "v"(after)); return r; }
#define KEEP(x) asm volatile("" : "+v"(x))
DI void softmax16(const f32x16& p, u32x4& o0, u32x4& o1) {
  const float m0 = fmaxf(p[0], p[15]);
  const float m = max3(max3(max3(m0, p[1], p[2], m0), max3(p[3], p[4], p[5], m0), max3(p[6], p[7], p[8], m0), m0), max3(p[9], p[10], p[11], m0), max3(p[12], p[13], p[14], m0), m0);
  const float a = -m * LOG2E;
  float e[16];
#pragma unroll
  for (int r = 0; r < 16; ++r) e[r] = __builtin_amdgcn_exp2f(__builtin_fmaf(p[r], LOG2E, a));
  float s[8];
#pragma unroll
  for (int r = 0; r < 8; ++r) s[r] = e[r] + e[r + 8];
  const float inv = __builtin_amdgcn_rcpf(((s[0] + s[4]) + (s[2] + s[6])) + ((s[1] + s[5]) + (s[3] + s[7])));
#pragma unroll
  for (int r = 0; r < 4; ++r) { o0[r] = pk2(e[2 * r] * inv, e[2 * r + 1] * inv); o1[r] = pk2(e[8 + 2 * r] * inv, e[9 + 2 * r] * inv); }
}
DI u32x2 glu4(f32x4 g, f32x4 u) {
  f32x4 h;
#pragma unroll
  for (int r = 0; r < 4; ++r) h[r] = u[r] * __builtin_amdgcn_rcpf(1.0f + __builtin_amdgcn_exp2f(-LOG2E * g[r]));
  return pk4(h);
}
DI u32x4 mk_frag(u32x2 t0, u32x2 t1) {
  auto r0 = __builtin_amdgcn_permlane16_swap(t0[0], t1[0], false, false);
  auto r1 = __builtin_amdgcn_permlane16_swap(t0[1], t1[1], false, false);
  return (u32x4){r0[0], r1[0], r0[1], r1[1]};
}
DI void poll_flags(const char* ws, unsigned off, int n, u64 tag, int lane) {
  GAS u64* f = (GAS u64*)(ws + off + (lane < n ? lane : 0) * 128);
#pragma unroll 1
  for (unsigned spins = 0; spins < SPIN_CAP; ++spins) {
    const u64 v = __hip_atomic_load(f, __ATOMIC_RELAXED, __HIP_MEMORY_SCOPE_AGENT);
    if (__all(v == tag)) break;
    __builtin_amdgcn_s_sleep(2);
  }
  asm volatile("" ::: "memory");
}
DI void set_flag(char* ws, unsigned off, u64 tag, int lane) {
  if (lane == 0) __hip_atomic_store((GAS u64*)(ws + off), tag, __ATOMIC_RELAXED, __HIP_MEMORY_SCOPE_AGENT);
  asm volatile("" ::: "memory");
}
#define STAMP(i) asm volatile("s_memrealtime %0\n\ts_waitcnt lgkmcnt(0)" : "=s"(stamps[i])::"memory")
#define STAMP_ARG , u64 (&stamps)[32]
#define STAMP_PASS , stamps

struct Ptrs { const char *x, *wqkv, *wphi, *wo, *wg, *wu, *wd; char *out, *ws; const char* code; };

DI void w2_piece(const Ptrs& P, int i, int kq, int rh, unsigned voff) {
  int q = kq;
  asm volatile("" : "+s"(q));
  const char* src = i < 16 ? P.wd + (256 * q + 16 * i) * 1024
                  : i < 24 ? P.wd + (1024 + 128 * q + 16 * (i - 16)) * 1024
                  : i < 28 ? P.wd + (1536 + 64 * q + 16 * (i - 24)) * 1024
                           : P.wo + (128 * q + 16 * (i - 28)) * 1024;
  dma_s(voff, src, (i % 18) * 8192 + q * 2048 + rh * 1024);
}
DI unsigned w2_voff(int lane, int rh, int ct64) {
  const int r = lane >> 3;
  return (unsigned)((r + 8 * rh) * 1024 + (((lane & 7) ^ (((r >> 1) & 1) << 2)) * 16) + ct64 * 128);
}
DI void w2_regs(u32x4 (&wr)[18][2], int lane, int kq) {
  const int q4 = (lane & 15) >> 2, blk = (lane >> 4) & 1;
  const int w2r = kq * 2048 + (8 * (lane >> 5) + q4) * 128 + 8 * (lane & 3);
  const int w2a = w2r + ((blk ^ (q4 & 2)) * 32), w2b = w2r + (((2 + blk) ^ (q4 & 2)) * 32);
#pragma unroll
  for (int i = 0; i < 18; ++i) { wr[i][0] = tr16(w2a + i * 8192, 512); wr[i][1] = tr16(w2b + i * 8192, 512); }
}

template <bool TA>
DI void phase1(const Ptrs& P, int lane, int w, int p, int ab, int ct64, u64& tag_out, u32x4 (&wr)[18][2] STAMP_ARG) {
  const int wm = w >> 2, wn = w & 3;
  const int g = lane >> 4, t16 = lane & 15, h = lane >> 5, t32 = lane & 31;
  u32x2 nonce; unsigned wq[8];
  {
    const char* wsn = P.ws;
    const char* wph = P.wphi + (4 * ab + wn) * 512;
    const unsigned z = 0, vo = (unsigned)(h * 256 + ((t32 & 3) + 4 * (t32 >> 3)) * 2);
    asm volatile("global_load_dwordx2 %0, %1, %2 sc1" : "=v"(nonce) : "v"(z), "s"(wsn) : "memory");
    asm volatile("global_load_ushort %0, %8, %9\n\tglobal_load_ushort %1, %8, %9 offset:32\n\t"
                 "global_load_ushort %2, %8, %9 offset:64\n\tglobal_load_ushort %3, %8, %9 offset:96\n\t"
                 "global_load_ushort %4, %8, %9 offset:128\n\tglobal_load_ushort %5, %8, %9 offset:160\n\t"
                 "global_load_ushort %6, %8, %9 offset:192\n\tglobal_load_ushort %7, %8, %9 offset:224"
                 : "=&v"(wq[0]), "=&v"(wq[1]), "=&v"(wq[2]), "=&v"(wq[3]), "=&v"(wq[4]), "=&v"(wq[5]), "=&v"(wq[6]), "=&v"(wq[7])
                 : "v"(vo), "s"(wph) : "memory");
  }
  u32x4 X[16];
  {
    const unsigned voff = (unsigned)((lane >> 3) * 1024 + (lane & 7) * 16);
    const char* xb = P.x + (p * 128 + 16 * w) * 1024;
#pragma unroll
    for (int i = 0; i < 16; ++i) {
      asm volatile("global_load_dwordx4 %0, %1, %2 offset:%3" : "=v"(X[i]) : "v"(voff), "s"(xb + (i & 1) * 8192), "i"((i >> 1) * 128) : "memory");
    }
  }
  {
    const char* cb = P.code;
    const int ln = w * 64 + lane;
    const unsigned vo = (unsigned)(ln < CODE_LINES ? ln : CODE_LINES - 1) * 128;
    asm volatile("s_mov_b32 m0, %2\n\ts_nop 0\n\tglobal_load_lds_dword %0, %1" ::"v"(vo), "s"(cb), "s"(SCR + w * 256) : "memory");
  }
  STAMP(16);
  asm volatile("s_barrier" ::: "memory");
  const int oct = w & 3;
  const int dr = lane >> 3;
  const int dsw = (lane & 7) ^ ((((dr >> 1) & 1) | ((oct & 1) << 1)) << 1);
  const unsigned vq = (unsigned)((8 * oct + dr) * 3072 + dsw * 16), vg = (unsigned)((8 * oct + dr) * 3584 + dsw * 16);
  const char* sp[5];
  const char* vp[2];
  unsigned vo[5]; int pitch[5]; unsigned lofs[5];
  {
    const char* gb[5];
    if (TA) { gb[0] = P.wqkv + (512 + 64 * ab) * 2; gb[1] = P.wqkv + (1024 + 64 * ab) * 2; gb[2] = P.wg + (16 + ab) * 128; gb[3] = P.wu + (16 + ab) * 128; gb[4] = gb[3]; }
    else { gb[0] = P.wqkv + 64 * ab * 2; gb[1] = P.wg + (2 * ab) * 128; gb[2] = P.wu + (2 * ab) * 128; gb[3] = P.wg + (2 * ab + 1) * 128; gb[4] = P.wu + (2 * ab + 1) * 128; }
#pragma unroll
    for (int m = 0; m < 5; ++m) {
      const int n0 = 8 * m, n1 = 8 * m + 4;
      const int G0 = (n0 % 20) / 4, G1 = (n1 % 20) / 4, S0 = n0 / 20, S1 = n1 / 20;
      const bool q0 = G0 == 0 || (TA && G0 == 1), q1 = G1 == 0 || (TA && G1 == 1);
      pitch[m] = wm ? (q1 ? 3072 : 3584) : (q0 ? 3072 : 3584);
      sp[m] = (wm ? gb[G1] : gb[G0]) + (wm ? S1 : S0) * 32 * pitch[m];
      vo[m] = wm ? (q1 ? vq : vg) : (q0 ? vq : vg);
      lofs[m] = (wm ? G1 : G0) * 4096 + oct * 1024;
    }
    if (TA) {
      const char* hp = (dsw < 4 ? P.wg : P.wu) + (24 + (ab >> 1)) * 128 + (ab & 1) * 64 + (8 * oct + dr) * 3584 + (dsw & 3) * 16;
      vp[0] = wm ? sp[2] + vo[2] : hp;
      vp[1] = wm ? hp + 32 * 3584 : sp[4] + vo[4];
    }
  }
  auto wpiece = [&](int D, int m) {
    const unsigned sa = ((2 * D + 4) % NWS) * WSLOT, sb = ((2 * D + 5) % NWS) * WSLOT;
    unsigned lb = lofs[m], hw = wm;
    asm volatile("" : "+s"(lb), "+s"(hw));
    const unsigned lds = lb + (m < 2 ? sa : m > 2 ? sb : sa + hw * (sb - sa));
    if (TA && (m == 2 || m == 4)) { dma_v(vp[m == 4], lds); vp[m == 4] += 64 * pitch[m]; }
    else { dma_s(vo[m], sp[m], lds); sp[m] += 64 * pitch[m]; }
  };
  float s1[2] = {0.f, 0.f}, s2[2] = {0.f, 0.f};
  const b16x2 ones2 = __builtin_bit_cast(b16x2, 0x3f803f80u);
#define XWAIT(N, i) asm volatile("s_waitcnt vmcnt(%4)" : "+v"(X[i]), "+v"(X[i + 1]), "+v"(X[i + 2]), "+v"(X[i + 3]) : "i"(N) : "memory")
#pragma unroll
  for (int q = 0; q < 4; ++q) {
#pragma unroll
    for (int n = 3 * q; n < 3 * q + 3; ++n) wpiece(n / 5, n % 5);
    if (q == 0) { XWAIT(16, 0); asm volatile("" : "+v"(nonce), "+v"(wq[0]), "+v"(wq[1]), "+v"(wq[2]), "+v"(wq[3]), "+v"(wq[4]), "+v"(wq[5]), "+v"(wq[6]), "+v"(wq[7])); }
    if (q == 1) XWAIT(15, 4);
    if (q == 2) XWAIT(14, 8);
    if (q == 3) XWAIT(13, 12);
    STAMP(17 + q);
#pragma unroll
    for (int i = 4 * q; i < 4 * q + 4; ++i)
#pragma unroll
      for (int e = 0; e < 4; ++e) {
        const unsigned xv = X[i][e];
        const b16x2 v = __builtin_bit_cast(b16x2, xv);
        s1[i & 1] = __builtin_amdgcn_fdot2_f32_bf16(v, ones2, s1[i & 1], false);
        s2[i & 1] = __builtin_amdgcn_fdot2_f32_bf16(v, v, s2[i & 1], false);
      }
    PIN();
  }
  STAMP(0);
  const u64 nn = ((u64)(unsigned)__builtin_amdgcn_readfirstlane(nonce[1]) << 32) | (unsigned)__builtin_amdgcn_readfirstlane(nonce[0]);
  const u64 tag = nn * 6364136223846793005ull + 1442695040888963407ull;
  tag_out = tag;
  u32x4 wphi = {wq[0] | (wq[1] << 16), wq[2] | (wq[3] << 16), wq[4] | (wq[5] << 16), wq[6] | (wq[7] << 16)};
  wphi ^= (t32 & 4) ? 0x80008000u : 0u;
  asm volatile("" : "+v"(wphi));
  float na[2], nb[2];
#pragma unroll
  for (int r = 0; r < 2; ++r) {
    float a = s1[r], b = s2[r];
    a = dpp_add<0xB1>(a); b = dpp_add<0xB1>(b);
    a = dpp_add<0x4E>(a); b = dpp_add<0x4E>(b);
    a = dpp_add<0x141>(a); b = dpp_add<0x141>(b);
    const float m = a * (1.0f / 512.0f);
    const float var = fmaxf(b * (1.0f / 512.0f) - m * m, 0.0f);
    na[r] = __builtin_amdgcn_rsqf(var + 1e-5f);
    nb[r] = -m * na[r];
  }
  const int xw = XOFF + (16 * w + dr) * 128 + (((lane & 7) ^ ((dr >> 1) & 3)) * 16);
  auto xaddr = [&](int i) { return ((xw + (i & 1) * 1024) ^ ((i & 1) * 64)) + (((i >> 1) + 1) & 1) * XSLOT; };
  auto xwrite = [&](int i) {
    u32x4 o;
#pragma unroll
    for (int e = 0; e < 4; ++e) o[e] = norm2(X[i][e], na[i & 1], nb[i & 1]);
    lds_w128(xaddr(i), o);
  };
  const int q4 = t16 >> 2, p4 = lane & 3;
  const int fsw = ((q4 >> 1) & 1) | ((g & 1) << 1);
  const int trb = g * 1024 + q4 * 128 + 8 * p4;
  const int tr0 = trb + ((wn ^ fsw) * 32);
  const int trg = trb + (((wn >> 1) ^ fsw) * 32) + 4 * 4096, tru = trb + (((2 + (wn >> 1)) ^ fsw) * 32) + 4 * 4096;
  const int xr = XOFF + (64 * wm + t16) * 128 + ((g ^ (t16 >> 1)) * 16);
  const int pr = TA ? (wn & 1) : 0;
  const int xrA = xr + pr * 4096, xrB = xr + (1 - pr) * 4096;
  const unsigned v2 = w2_voff(lane, wm, ct64);
  constexpr int NWF = TA ? 6 : 5, NRD = 2 * NWF + 4;
  u32x2 wf[2][NWF][2];
  u32x4 xf[2][4];
  auto fread = [&](int s, int r) {
    const int slot = ((s + 4) % NWS) * WSLOT, b = s & 1, xs = (((s >> 1) + 1) & 1) * XSLOT;
    if (r < 4) xf[b][r] = lds_r128(((r < 2 ? xrA : xrB) ^ (b * 64)) + xs + (r & 1) * 2048);
    else {
      const int G = (r - 4) >> 1, t = (r - 4) & 1;
      wf[b][G][t] = tr8(((TA && G == 4) ? trg : (TA && G == 5) ? tru : tr0 + G * 4096) + slot + t * 512);
    }
  };
  f32x4 acc[5][4];
#pragma unroll
  for (int a = 0; a < 5; ++a)
#pragma unroll
    for (int b = 0; b < 4; ++b) acc[a][b] = (f32x4){0.f, 0.f, 0.f, 0.f};
  xwrite(0); xwrite(1);
  wait_vm_bar<9>();
  STAMP(1);
#pragma unroll
  for (int r = 0; r < NRD; ++r) fread(0, r);
  xwrite(2);
#pragma unroll
  for (int s = 0; s < 16; ++s) {
    if (s <= 10) wait_vm_bar<7>(); else if (s <= 13) wait_vm_bar<8>(); else if (s == 14) wait_vm_bar<10>();
    PIN();
    const int b = s & 1;
    u32x4 xo;
#pragma unroll
    for (int k = 0; k < 20; ++k) {
      const int G = k >> 2, rt = k & 3;
      if (TA && G == 4) {
        const int gi = 4 + (rt >> 1);
        acc[4][rt] = mfma16((u32x4){wf[b][gi][0][0], wf[b][gi][0][1], wf[b][gi][1][0], wf[b][gi][1][1]}, xf[b][rt & 1], acc[4][rt]);
      } else {
        const u32x4 wv = {wf[b][G][0][0], wf[b][G][0][1], wf[b][G][1][0], wf[b][G][1][1]};
        acc[G][rt] = (TA && G == 1) ? mfma16(xf[b][rt], wv, acc[G][rt]) : mfma16(wv, xf[b][rt], acc[G][rt]);
      }
      asm volatile("" : "+v"(acc[G][rt]));
      PIN();
      if (s <= 10) {
        if (s & 1) { if (k == 0) wpiece((s + 5) / 2, 0); if (k == 6) wpiece((s + 5) / 2, 1); }
        else { if (k == 0) wpiece((s + 4) / 2, 2); if (k == 5) wpiece((s + 4) / 2, 3); if (k == 10) wpiece((s + 4) / 2, 4); }
      }
      if (s == 10 && k == 15) w2_piece(P, 17, wn, wm, v2);
      if (s == 11) { if (k == 0) w2_piece(P, 0, wn, wm, v2); if (k == 6) w2_piece(P, 1, wn, wm, v2); }
      if (s == 12) { if (k == 0) w2_piece(P, 2, wn, wm, v2); if (k == 5) w2_piece(P, 3, wn, wm, v2); if (k == 10) w2_piece(P, 4, wn, wm, v2); }
      if (s == 13) { if (k == 0) w2_piece(P, 5, wn, wm, v2); if (k == 5) w2_piece(P, 6, wn, wm, v2); if (k == 10) w2_piece(P, 15, wn, wm, v2); if (k == 15) w2_piece(P, 16, wn, wm, v2); }
      if (s == 14) { if (k == 0) w2_piece(P, 7, wn, wm, v2); if (k == 5) w2_piece(P, 8, wn, wm, v2); if (k == 10) w2_piece(P, 9, wn, wm, v2); }
      if (s < 15 && k >= 1 && k - 1 < NRD) fread(s + 1, k - 1);
      if (s + 3 < 16) {
        const int i = s + 3;
        if ((k & 3) == 3 && k < 16) xo[k >> 2] = norm2(X[i][k >> 2], na[i & 1], nb[i & 1]);
        if (k == 16) lds_w128(xaddr(i), xo);
      }
      if (s == 15 && k == 0) {
        bar();
#pragma unroll
        for (int i = 10; i < 15; ++i) w2_piece(P, i, wn, wm, v2);
      }
      PIN();
    }
  }
  STAMP(2);
  const unsigned abase = WS_AB + (unsigned)(p * 4 + wm * 2) * (144 * 1024);
  const unsigned l16 = lane * 16;
  const int scr = SCR + w * 2048, scr_o = SCR + (w ^ 4) * 2048;
  const unsigned fh = WS_FH + ((((p * 2) * 4 + (ab >> 1)) * 2 + wm) * 16 + (ab & 1) * 4 + wn) * 128;
  if (TA) {
    f32x16 kv = zero16();
    const int iw = scr + t32 * 64, sw = (t32 >> 1) & 3;
    const int rr = 8 * h + q4, uu = (p4 & 1) * 4 + 2 * (g & 1) + (p4 >> 1);
    const int ir0 = scr + rr * 64 + (((uu >> 1) ^ ((rr >> 1) & 3)) * 16) + (uu & 1) * 8;
    const int ir1 = scr + (rr + 4) * 64 + (((uu >> 1) ^ (((rr + 4) >> 1) & 3)) * 16) + (uu & 1) * 8;
#pragma unroll
    for (int pp = 0; pp < 2; ++pp) {
      u32x4 f0, f1;
      softmax16(mfma32(wphi, mk_frag(pk4(acc[0][2 * pp]), pk4(acc[0][2 * pp + 1])), zero16()), f0, f1);
      lds_w128(iw + (((2 * h) ^ sw) * 16), f0);
      lds_w128(iw + (((2 * h + 1) ^ sw) * 16), f1);
#pragma unroll
      for (int e = 0; e < 2; ++e) {
        const u32x2 a0 = tr8(ir0 + e * 1024), a1 = tr8(ir1 + e * 1024);
        const u32x4 vb = mk_frag(pk4(acc[1][2 * pp + e]), (u32x2){0x3f803f80u, 0x3f803f80u});
        kv = mfma32((u32x4){a0[0], a0[1], a1[0], a1[1]}, vb, kv);
      }
    }
    STAMP(11);
    f32x4 k0, k1;
    if (wm == 0) {
      lds_wf(scr + l16, (f32x4){kv[8], kv[9], kv[10], kv[11]}); lds_wf(scr + 1024 + l16, (f32x4){kv[12], kv[13], kv[14], kv[15]});
      k0 = (f32x4){kv[0], kv[1], kv[2], kv[3]}; k1 = (f32x4){kv[4], kv[5], kv[6], kv[7]};
    } else {
      lds_wf(scr + l16, (f32x4){kv[0], kv[1], kv[2], kv[3]}); lds_wf(scr + 1024 + l16, (f32x4){kv[4], kv[5], kv[6], kv[7]});
      k0 = (f32x4){kv[8], kv[9], kv[10], kv[11]}; k1 = (f32x4){kv[12], kv[13], kv[14], kv[15]};
    }
    bar();
    k0 += lds_rf(scr_o + l16); k1 += lds_rf(scr_o + 1024 + l16);
    {
      const u32x2 a = pk4(k0), b = pk4(k1);
      const unsigned id = (ab * 4 + wn) * 16 + p;
      const unsigned off = t32 < 16 ? WS_KVP + id * 1024 + wm * 512 + h * 256 + t16 * 16 : WS_ZP + id * 64 + h * 32 + wm * 16;
      if (t32 <= 16) store_sc1(P.ws, off, (u32x4){a[0], a[1], b[0], b[1]});
    }
    asm volatile("" ::: "memory");
    const u32x4 fr0 = mk_frag(glu4(acc[2][0], acc[3][0]), glu4(acc[2][1], acc[3][1]));
    const u32x4 fr1 = mk_frag(glu4(acc[2][2], acc[3][2]), glu4(acc[2][3], acc[3][3]));
    store_sc1(P.ws + (abase + pr * (144 * 1024) + (64 + 4 * ab + wn) * 1024), l16, fr0);
    store_sc1(P.ws + (abase + (1 - pr) * (144 * 1024) + (64 + 4 * ab + wn) * 1024), l16, fr1);
    wait_vm<2>();
    set_flag(P.ws, WS_FKV + (((ab * 4 + wn) * 16 + p) * 2 + wm) * 128, tag, lane);
    STAMP(3);
    const u32x4 fr2 = mk_frag(glu4(acc[4][0], acc[4][2]), glu4(acc[4][1], acc[4][3]));
    store_sc1(P.ws + (abase + pr * (144 * 1024) + (96 + 2 * ab + (wn >> 1)) * 1024), l16, fr2);
    bar();
    w2_regs(wr, lane, wn);
    wait_vm<0>();
    set_flag(P.ws, fh + 8 * 128, tag, lane);
    STAMP(4);
    bar();
#pragma unroll
    for (int i = 18; i < 27; ++i) w2_piece(P, i, wn, wm, v2);
  } else {
#pragma unroll
    for (int h2 = 0; h2 < 2; ++h2)
#pragma unroll
      for (int pp = 0; pp < 2; ++pp) {
        const u32x4 fr4 = mk_frag(glu4(acc[1 + 2 * h2][2 * pp], acc[2 + 2 * h2][2 * pp]), glu4(acc[1 + 2 * h2][2 * pp + 1], acc[2 + 2 * h2][2 * pp + 1]));
        store_sc1(P.ws + (abase + pp * (144 * 1024) + (8 * ab + 4 * h2 + wn) * 1024), l16, fr4);
      }
    STAMP(11);
    wait_vm_bar<4>();
    w2_regs(wr, lane, wn);
    u32x4 ph[2][2];
#pragma unroll
    for (int pp = 0; pp < 2; ++pp)
      softmax16(mfma32(wphi, mk_frag(pk4(acc[0][2 * pp]), pk4(acc[0][2 * pp + 1])), zero16()), ph[pp][0], ph[pp][1]);
    KEEP(ph[0][0]); KEEP(ph[0][1]); KEEP(ph[1][0]); KEEP(ph[1][1]);
    wait_vm<0>();
    set_flag(P.ws, fh, tag, lane);
    STAMP(3);
    bar();
    poll_flags(P.ws, WS_FKV + (ab * 4 + wn) * 32 * 128, 32, tag, lane);
    STAMP(12);
    u32x4 part[8], zq;
    {
      const char* kb = P.ws + WS_KVP + ((ab * 4 + wn) * 16 + 8 * wm) * 1024;
      const char* zb = P.ws + WS_ZP + (ab * 4 + wn) * 1024;
      const unsigned vk = (g & 1) * 4096 + h * 256 + t16 * 16, vz = t16 * 64 + g * 16;
      asm volatile("global_load_dwordx4 %0, %1, %2 sc1" : "=v"(zq) : "v"(vz), "s"(zb) : "memory");
#pragma unroll
      for (int i = 0; i < 8; ++i) asm volatile("global_load_dwordx4 %0, %1, %2 offset:%3 sc1" : "=v"(part[i]) : "v"(vk), "s"(kb), "i"(i * 512) : "memory");
    }
#pragma unroll
    for (int i = 18; i < 27; ++i) w2_piece(P, i, wn, wm, v2);
    STAMP(21);
    asm volatile("s_waitcnt vmcnt(17)" : "+v"(zq)::"memory");
    STAMP(22);
    float z[8];
#pragma unroll
    for (int e = 0; e < 4; ++e) { z[2 * e] = lo16(zq[e]); z[2 * e + 1] = hi16(zq[e]); }
#pragma unroll
    for (int e = 0; e < 8; ++e) { z[e] = dpp_add<0xB1>(z[e]); z[e] = dpp_add<0x4E>(z[e]); z[e] = dpp_add<0x141>(z[e]); z[e] = dpp_add<0x140>(z[e]); }
    u32x4 zp = {pk2(z[0], z[1]), pk2(z[2], z[3]), pk2(z[4], z[5]), pk2(z[6], z[7])};
    KEEP(zp);
    asm volatile("s_waitcnt vmcnt(9)" : "+v"(part[0]), "+v"(part[1]), "+v"(part[2]), "+v"(part[3]), "+v"(part[4]), "+v"(part[5]), "+v"(part[6]), "+v"(part[7])::"memory");
    STAMP(23);
    f32x4 k0, k1;
#pragma unroll
    for (int e = 0; e < 8; ++e) {
      float a = 0.f, b = 0.f;
#pragma unroll
      for (int i = 0; i < 4; ++i) {
        const unsigned ua = part[2 * i][e >> 1], ub = part[2 * i + 1][e >> 1];
        a += (e & 1) ? hi16(ua) : lo16(ua); b += (e & 1) ? hi16(ub) : lo16(ub);
      }
      auto r = __builtin_amdgcn_permlane16_swap(ubits(a), ubits(b), false, false);
      const float v = fbits(r[0]) + fbits(r[1]);
      if (e < 4) k0[e] = v; else k1[e - 4] = v;
    }
    lds_wf(scr + l16, k0); lds_wf(scr + 1024 + l16, k1);
    bar();
    k0 += lds_rf(scr_o + l16); k1 += lds_rf(scr_o + 1024 + l16);
    u32x4 a0, a1;
    {
      const u32x2 ka = pk4(k0), kb = pk4(k1);
      const unsigned kp[4] = {ka[0], ka[1], kb[0], kb[1]};
#pragma unroll
      for (int e = 0; e < 4; ++e) {
        auto r = __builtin_amdgcn_permlane16_swap(kp[e], zp[e], false, false);
        a0[e] = r[0]; a1[e] = r[1];
      }
    }
#pragma unroll
    for (int pp = 0; pp < 2; ++pp) {
      const f32x16 nd = mfma32(a1, ph[pp][1], mfma32(a0, ph[pp][0], zero16()));
      const float r = __builtin_amdgcn_rcpf(nd[8] + 1e-6f);
      auto s0 = __builtin_amdgcn_permlane32_swap(pk2(nd[0] * r, nd[1] * r), pk2(nd[4] * r, nd[5] * r), false, false);
      auto s1 = __builtin_amdgcn_permlane32_swap(pk2(nd[2] * r, nd[3] * r), pk2(nd[6] * r, nd[7] * r), false, false);
      store_sc1(P.ws + (abase + pp * (144 * 1024) + (112 + 4 * ab + wn) * 1024), l16, (u32x4){s0[0], s1[0], s0[1], s1[1]});
    }
    STAMP(24);
    wait_vm<0>();
    set_flag(P.ws, fh + 4 * 2 * 16 * 128, tag, lane);
    STAMP(4);
  }
}

__global__ __launch_bounds__(512, 2) void fused_block(const char* x, const char* wqkv, const char* wphi, const char* wo, const char* wg,
                                                      const char* wu, const char* wd, char* out, char* ws) {
  u64 pc; asm volatile("s_getpc_b64 %0" : "=s"(pc));
  const int lane = threadIdx.x & 63;
  int w = __builtin_amdgcn_readfirstlane(threadIdx.x >> 6);
  const int b = blockIdx.x, xcd = b & 7, bi = b >> 3;
  const Ptrs P = {x, wqkv, wphi, wo, wg, wu, wd, out, ws, (const char*)(pc & ~(u64)0xff)};
  const int p = (xcd & 1) * 8 + (bi & 7), jj = bi >> 3, ab = (xcd >> 1) * 2 + (jj & 1);
  int t64 = xcd * 4 + (bi >> 3), ct64 = bi & 7;
  u64 stamps[32];
#pragma unroll
  for (int i = 0; i < 32; ++i) stamps[i] = 0;
  asm volatile("s_memtime %0\n\ts_waitcnt lgkmcnt(0)" : "=s"(stamps[9])::"memory");
  STAMP(7);
  u64 tag;
  u32x4 wr[18][2];
  if (jj < 2) phase1<true>(P, lane, w, p, ab, ct64, tag, wr STAMP_PASS);
  else phase1<false>(P, lane, w, p, ab, ct64, tag, wr STAMP_PASS);

  asm volatile("" : "+s"(w), "+s"(t64), "+s"(ct64));
  const int kq = w & 3, rh = w >> 2;
  const int h = lane >> 5, q4 = (lane & 15) >> 2, blk = (lane >> 4) & 1;
  const int w2r = kq * 2048 + (8 * h + q4) * 128 + 8 * (lane & 3);
  const int w2a = w2r + ((blk ^ (q4 & 2)) * 32), w2b = w2r + (((2 + blk) ^ (q4 & 2)) * 32);
  const unsigned v2 = w2_voff(lane, rh, ct64);
  const char* ab0 = ws + WS_AB + (unsigned)(t64 * 2 + rh) * (144 * 1024);
  const unsigned l16 = lane * 16;
  STAMP(5);
  u32x4 res;
  {
    const char* xs = x + (t64 * 64 + rh * 32) * 1024 + ct64 * 128 + kq * 32;
    const unsigned vo = (lane & 31) * 1024 + h * 16;
    asm volatile("global_load_dwordx4 %0, %1, %2" : "=v"(res) : "v"(vo), "s"(xs) : "memory");
  }
  f32x16 acc0 = zero16(), acc1 = zero16();
  u32x4 af[16];
  auto aload = [&](int i) {
    int q = kq;
    asm volatile("" : "+s"(q));
    const int i4 = i & ~3;
    const int ks = i4 < 16 ? 16 * q + i4 : i4 < 24 ? 64 + 8 * q + (i4 - 16) : i4 < 28 ? 96 + 4 * q : 112 + 8 * q + (i4 - 28);
    const char* s = ab0 + ks * 1024;
    asm volatile("global_load_dwordx4 %0, %1, %2 offset:%3 sc1" : "=v"(af[i & 15]) : "v"(l16), "s"(s), "i"((i & 3) * 1024) : "memory");
  };
  const unsigned fh = WS_FH + ((((t64 >> 1) * 2) * 4 + kq) * 2 + (t64 & 1)) * 16 * 128;
  poll_flags(ws, fh, 16, tag, lane);
  asm volatile("" : "+v"(res));
  STAMP(13);
#pragma unroll
  for (int i = 0; i < 16; ++i) aload(i);
#pragma unroll
  for (int i = 27; i < 36; ++i) w2_piece(P, i, kq, rh, v2);
  u32x2 fl;
  u32x4 wn0, wn1;
#pragma unroll
  for (int i = 0; i < 36; ++i) {
    if (i == 1) STAMP(25);
    if (i == 12) {
      STAMP(26);
      asm volatile("s_waitcnt vmcnt(6)" : "+v"(fl)::"memory");
      const u64 v = ((u64)fl[1] << 32) | fl[0];
      if (!__all(v == tag)) poll_flags(ws, fh + 4 * 2 * 16 * 128, 8, tag, lane);
    }
    if (i == 12) STAMP(27);
    if (i == 28) STAMP(28);
    if (i == 17 || i == 30) bar();
    if (i < 16) asm volatile("s_waitcnt vmcnt(24)" : "+v"(af[i & 15])::"memory");
    else if (i < 20) asm volatile("s_waitcnt vmcnt(15)" : "+v"(af[i & 15])::"memory");
    else asm volatile("s_waitcnt vmcnt(%1)" : "+v"(af[i & 15]) : "i"(35 - i) : "memory");
    const u32x4 a = af[i & 15];
    const u32x4 w0 = i < 18 ? wr[i < 18 ? i : 0][0] : wn0, w1 = i < 18 ? wr[i < 18 ? i : 0][1] : wn1;
    acc0 = mfma32(w0, a, acc0);
    asm volatile("" : "+v"(acc0));
    if (i >= 17 && i < 35) wn0 = tr16(w2a + (i - 17) * 8192, 512);
    acc1 = mfma32(w1, a, acc1);
    asm volatile("" : "+v"(acc1));
    if (i >= 17 && i < 35) wn1 = tr16(w2b + (i - 17) * 8192, 512);
    PIN();
    if (i + 16 < 36) aload(i + 16);
    if (i == 5) {
      const char* fb = ws + fh + 4 * 2 * 16 * 128;
      const unsigned vo = (lane & 7) * 128;
      asm volatile("global_load_dwordx2 %0, %1, %2 sc1" : "=v"(fl) : "v"(vo), "s"(fb) : "memory");
    }
    PIN();
  }
  STAMP(6);
  float v[8];
#pragma unroll
  for (int d = 0; d < 4; ++d) {
    const f32x16& s = d < 2 ? acc0 : acc1;
    const int o = (d & 1) * 8;
    const int dst = ((rh * 4 + d) * 4 + kq) * 2048 + l16;
    if (d != kq) {
      lds_wf(dst, (f32x4){s[o], s[o + 1], s[o + 2], s[o + 3]});
      lds_wf(dst + 1024, (f32x4){s[o + 4], s[o + 5], s[o + 6], s[o + 7]});
    } else {
#pragma unroll
      for (int e = 0; e < 8; ++e) v[e] = s[o + e];
    }
  }
  bar();
#pragma unroll
  for (int n = 1; n < 4; ++n) {
    const int src = ((rh * 4 + kq) * 4 + (kq ^ n)) * 2048 + l16;
    const f32x4 a = lds_rf(src), c = lds_rf(src + 1024);
#pragma unroll
    for (int e = 0; e < 4; ++e) { v[e] += a[e]; v[4 + e] += c[e]; }
  }
#pragma unroll
  for (int e = 0; e < 4; ++e) {
    auto r = __builtin_amdgcn_permlane32_swap(ubits(v[e]), ubits(v[4 + e]), false, false);
    v[e] = fbits(r[0]); v[4 + e] = fbits(r[1]);
  }
  u32x4 o;
#pragma unroll
  for (int e = 0; e < 4; ++e) o[e] = pk2(v[2 * e] + lo16(res[e]), v[2 * e + 1] + hi16(res[e]));
  *(GAS u32x4*)(out + (t64 * 64 + rh * 32 + (lane & 31)) * 1024 + ct64 * 128 + kq * 32 + h * 16) = o;
  if (b == 0) {
    if (w < 4) {
      const int id = w * 64 + lane, pp = id >> 4, ty = (id >> 3) & 1, aa = id & 7;
      GAS u64* f = (GAS u64*)(ws + WS_FH + ((((pp * 2) * 4 + (aa >> 1)) * 2) * 16 + ty * 8 + (aa & 1) * 4) * 128);
#pragma unroll 1
      for (unsigned spins = 0; spins < SPIN_CAP; ++spins) {
        if (__all(__hip_atomic_load(f, __ATOMIC_RELAXED, __HIP_MEMORY_SCOPE_AGENT) == tag)) break;
        __builtin_amdgcn_s_sleep(2);
      }
    }
    bar();
    if (threadIdx.x == 0) __hip_atomic_store((GAS u64*)(ws + WS_NONCE), tag, __ATOMIC_RELAXED, __HIP_MEMORY_SCOPE_AGENT);
  }
  STAMP(8);
  if (threadIdx.x == 0) { *(GAS u64*)(ws + WS_END + 4096 + b * 16) = stamps[7]; *(GAS u64*)(ws + WS_END + 4096 + b * 16 + 8) = stamps[8]; }
}

__global__ void k_pre(char* ws) { if (threadIdx.x == 0) *(GAS u64*)(ws + WS_END) = __builtin_amdgcn_s_memrealtime(); }
__global__ void k_post(char* ws) {
  const u64 t = __builtin_amdgcn_s_memrealtime();
  u64 mn = ~0ull, mx = 0;
  for (int i = 0; i < 256; ++i) { const u64 e = *(GAS u64*)(ws + WS_END + 4096 + i * 16), x = *(GAS u64*)(ws + WS_END + 4096 + i * 16 + 8); mn = e < mn ? e : mn; mx = x > mx ? x : mx; }
  const u64 pre = *(GAS u64*)(ws + WS_END);
  u64 d = GAPS == 0 ? 0 : GAPS == 1 ? 8 * (mn - pre) : GAPS == 2 ? 8 * (t - mx) : 2 * (mx - mn);
  if (d > 100000) d = 100000;
  const u64 t2 = t + 1000 + d;
  while (__builtin_amdgcn_s_memrealtime() < t2) __builtin_amdgcn_s_sleep(1);
}
extern "C" void kernel_launch(void* const* d_in, const int* in_sizes, int n_in, void* d_out, int out_size, void* d_ws, size_t ws_size,
                              hipStream_t stream) {
  (void)hipFuncSetAttribute((const void*)fused_block, hipFuncAttributeMaxDynamicSharedMemorySize, LDS_BYTES);
  k_pre<<<1, 64, 0, stream>>>((char*)d_ws);
  fused_block<<<256, 512, LDS_BYTES, stream>>>((const char*)d_in[0], (const char*)d_in[1], (const char*)d_in[2], (const char*)d_in[3],
                                               (const char*)d_in[5], (const char*)d_in[6], (const char*)d_in[7], (char*)d_out, (char*)d_ws);
  k_post<<<1, 64, 0, stream>>>((char*)d_ws);
}
```

```cpp
#include <hip/hip_runtime.h>
#include <stdint.h>

typedef __attribute__((ext_vector_type(8))) __bf16 b16x8;
typedef __attribute__((ext_vector_type(2))) __bf16 b16x2;
typedef __attribute__((ext_vector_type(4))) short s16x4;
typedef __attribute__((ext_vector_type(4))) float f32x4;
typedef __attribute__((ext_vector_type(2))) float f32x2;
typedef __attribute__((ext_vector_type(16))) float f32x16;
typedef __attribute__((ext_vector_type(4))) unsigned u32x4;
typedef __attribute__((ext_vector_type(2))) unsigned u32x2;
typedef unsigned long long u64;
#define LAS __attribute__((address_space(3)))
#define GAS __attribute__((address_space(1)))
#define DI __device__ __forceinline__

#define STAMPS 0

constexpr unsigned WS_NONCE = 0;
constexpr unsigned WS_FKV = 64 << 10;
constexpr unsigned WS_FH = 256 << 10;
constexpr unsigned WS_KVP = 768 << 10;
constexpr unsigned WS_ZP = 1280 << 10;
constexpr unsigned WS_AB = 2u << 20;
constexpr unsigned WS_END = WS_AB + 64u * 144u * 1024u;
constexpr int WSLOT = 20480, NWS = 5, XOFF = 102400, XSLOT = 16384;
constexpr int SCR = 147456, LDS_BYTES = 163840;
constexpr float LOG2E = 1.4426950408889634f;
constexpr unsigned SPIN_CAP = 1u << 15;
constexpr int CODE_LINES = 288;

extern __shared__ __attribute__((aligned(16))) char smem[];

DI unsigned pk2(float a, float b) { f32x2 f = {a, b}; return __builtin_bit_cast(unsigned, __builtin_convertvector(f, b16x2)); }
DI u32x2 pk4(f32x4 v) { return (u32x2){pk2(v[0], v[1]), pk2(v[2], v[3])}; }
DI float lo16(unsigned u) { return __builtin_bit_cast(float, u << 16); }
DI float hi16(unsigned u) { return __builtin_bit_cast(float, u & 0xffff0000u); }
DI float fbits(unsigned u) { return __builtin_bit_cast(float, u); }
DI unsigned ubits(float f) { return __builtin_bit_cast(unsigned, f); }
DI unsigned norm2(unsigned x, float a, float b) {
  const float lo = __builtin_fmaf(lo16(x), a, b);
  float hi = __builtin_fmaf(hi16(x), a, b);
  asm("" : "+v"(hi));
  return pk2(lo, hi);
}
DI f32x4 mfma16(u32x4 a, u32x4 b, f32x4 c) {
  return __builtin_amdgcn_mfma_f32_16x16x32_bf16(__builtin_bit_cast(b16x8, a), __builtin_bit_cast(b16x8, b), c, 0, 0, 0);
}
DI f32x16 mfma32(u32x4 a, u32x4 b, f32x16 c) {
  return __builtin_amdgcn_mfma_f32_32x32x16_bf16(__builtin_bit_cast(b16x8, a), __builtin_bit_cast(b16x8, b), c, 0, 0, 0);
}
DI f32x16 zero16() { f32x16 z; for (int e = 0; e < 16; ++e) z[e] = 0.f; return z; }
DI u32x2 tr8(int off) {
  return __builtin_bit_cast(u32x2, __builtin_amdgcn_ds_read_tr16_b64_v4i16((LAS s16x4*)(smem + off)));
}
DI u32x4 tr16(int off, int second) { u32x2 a = tr8(off), b = tr8(off + second); return (u32x4){a[0], a[1], b[0], b[1]}; }
DI u32x4 lds_r128(int off) { return *(LAS u32x4*)(smem + off); }
DI f32x4 lds_rf(int off) { return *(LAS f32x4*)(smem + off); }
DI void lds_w128(int off, u32x4 v) { *(LAS u32x4*)(smem + off) = v; }
DI void lds_wf(int off, f32x4 v) { *(LAS f32x4*)(smem + off) = v; }
DI void dma_s(unsigned voff, const char* sbase, unsigned lds) {
  asm volatile("s_mov_b32 m0, %2\n\ts_nop 0\n\tglobal_load_lds_dwordx4 %0, %1" ::"v"(voff), "s"(sbase), "s"(lds) : "memory");
}
DI void dma_v(const char* vaddr, unsigned lds) {
  asm volatile("s_mov_b32 m0, %1\n\ts_nop 0\n\tglobal_load_lds_dwordx4 %0, off" ::"v"(vaddr), "s"(lds) : "memory");
}
DI void store_sc1(char* sbase, unsigned voff, u32x4 v) {
  asm volatile("global_store_dwordx4 %0, %1, %2 sc1\n\ts_nop 1" ::"v"(voff), "v"(v), "s"(sbase) : "memory");
}
template <int N> DI void wait_vm() { asm volatile("s_waitcnt vmcnt(%0)" ::"i"(N) : "memory"); }
template <int N> DI void wait_vm_bar() {
  asm volatile("" ::: "memory");
  __builtin_amdgcn_s_waitcnt((N & 15) | (7 << 4) | ((N >> 4) << 14));
  asm volatile("s_barrier" ::: "memory");
}
DI void bar() { wait_vm_bar<63>(); }
#define PIN() __builtin_amdgcn_sched_barrier(0)
template <int CTRL> DI float dpp_add(float v) {
  return v + fbits(__builtin_amdgcn_update_dpp(0u, ubits(v), CTRL, 0xf, 0xf, true));
}
DI float max3(float a, float b, float c, float after) { float r; asm("v_max3_f32 %0, %1, %2, %3" : "=v"(r) : "v"(a), "v"(b), "v"(c), "v"(after)); return r; }
#define KEEP(x) asm volatile("" : "+v"(x))
DI void softmax16(const f32x16& p, u32x4& o0, u32x4& o1) {
  const float m0 = fmaxf(p[0], p[15]);
  const float m = max3(max3(max3(m0, p[1], p[2], m0), max3(p[3], p[4], p[5], m0), max3(p[6], p[7], p[8], m0), m0), max3(p[9], p[10], p[11], m0), max3(p[12], p[13], p[14], m0), m0);
  const float a = -m * LOG2E;
  float e[16];
#pragma unroll
  for (int r = 0; r < 16; ++r) e[r] = __builtin_amdgcn_exp2f(__builtin_fmaf(p[r], LOG2E, a));
  float s[8];
#pragma unroll
  for (int r = 0; r < 8; ++r) s[r] = e[r] + e[r + 8];
  const float inv = __builtin_amdgcn_rcpf(((s[0] + s[4]) + (s[2] + s[6])) + ((s[1] + s[5]) + (s[3] + s[7])));
#pragma unroll
  for (int r = 0; r < 4; ++r) { o0[r] = pk2(e[2 * r] * inv, e[2 * r + 1] * inv); o1[r] = pk2(e[8 + 2 * r] * inv, e[9 + 2 * r] * inv); }
}
DI u32x2 glu4(f32x4 g, f32x4 u) {
  f32x4 h;
#pragma unroll
  for (int r = 0; r < 4; ++r) h[r] = u[r] * __builtin_amdgcn_rcpf(1.0f + __builtin_amdgcn_exp2f(-LOG2E * g[r]));
  return pk4(h);
}
DI u32x4 mk_frag(u32x2 t0, u32x2 t1) {
  auto r0 = __builtin_amdgcn_permlane16_swap(t0[0], t1[0], false, false);
  auto r1 = __builtin_amdgcn_permlane16_swap(t0[1], t1[1], false, false);
  return (u32x4){r0[0], r1[0], r0[1], r1[1]};
}
DI void poll_flags(const char* ws, unsigned off, int n, u64 tag, int lane) {
  GAS u64* f = (GAS u64*)(ws + off + (lane < n ? lane : 0) * 128);
#pragma unroll 1
  for (unsigned spins = 0; spins < SPIN_CAP; ++spins) {
    const u64 v = __hip_atomic_load(f, __ATOMIC_RELAXED, __HIP_MEMORY_SCOPE_AGENT);
    if (__all(v == tag)) break;
    __builtin_amdgcn_s_sleep(2);
  }
  asm volatile("" ::: "memory");
}
DI void set_flag(char* ws, unsigned off, u64 tag, int lane) {
  if (lane == 0) __hip_atomic_store((GAS u64*)(ws + off), tag, __ATOMIC_RELAXED, __HIP_MEMORY_SCOPE_AGENT);
  asm volatile("" ::: "memory");
}
#define STAMP(i)
#define STAMP_ARG
#define STAMP_PASS

struct Ptrs { const char *x, *wqkv, *wphi, *wo, *wg, *wu, *wd; char *out, *ws; const char* code; };

DI void w2_piece(const Ptrs& P, int i, int kq, int rh, unsigned voff) {
  int q = kq;
  asm volatile("" : "+s"(q));
  const char* src = i < 16 ? P.wd + (256 * q + 16 * i) * 1024
                  : i < 24 ? P.wd + (1024 + 128 * q + 16 * (i - 16)) * 1024
                  : i < 28 ? P.wd + (1536 + 64 * q + 16 * (i - 24)) * 1024
                           : P.wo + (128 * q + 16 * (i - 28)) * 1024;
  dma_s(voff, src, (i % 18) * 8192 + q * 2048 + rh * 1024);
}
DI unsigned w2_voff(int lane, int rh, int ct64) {
  const int r = lane >> 3;
  return (unsigned)((r + 8 * rh) * 1024 + (((lane & 7) ^ (((r >> 1) & 1) << 2)) * 16) + ct64 * 128);
}
DI void w2_regs(u32x4 (&wr)[18][2], int lane, int kq) {
  const int q4 = (lane & 15) >> 2, blk = (lane >> 4) & 1;
  const int w2r = kq * 2048 + (8 * (lane >> 5) + q4) * 128 + 8 * (lane & 3);
  const int w2a = w2r + ((blk ^ (q4 & 2)) * 32), w2b = w2r + (((2 + blk) ^ (q4 & 2)) * 32);
#pragma unroll
  for (int i = 0; i < 18; ++i) { wr[i][0] = tr16(w2a + i * 8192, 512); wr[i][1] = tr16(w2b + i * 8192, 512); }
}

template <bool TA>
DI void phase1(const Ptrs& P, int lane, int w, int p, int ab, int ct64, u64& tag_out, u32x4 (&wr)[18][2] STAMP_ARG) {
  const int wm = w >> 2, wn = w & 3;
  const int g = lane >> 4, t16 = lane & 15, h = lane >> 5, t32 = lane & 31;
  u32x2 nonce; unsigned wq[8];
  {
    const char* wsn = P.ws;
    const char* wph = P.wphi + (4 * ab + wn) * 512;
    const unsigned z = 0, vo = (unsigned)(h * 256 + ((t32 & 3) + 4 * (t32 >> 3)) * 2);
    asm volatile("global_load_dwordx2 %0, %1, %2 sc1" : "=v"(nonce) : "v"(z), "s"(wsn) : "memory");
    asm volatile("global_load_ushort %0, %8, %9\n\tglobal_load_ushort %1, %8, %9 offset:32\n\t"
                 "global_load_ushort %2, %8, %9 offset:64\n\tglobal_load_ushort %3, %8, %9 offset:96\n\t"
                 "global_load_ushort %4, %8, %9 offset:128\n\tglobal_load_ushort %5, %8, %9 offset:160\n\t"
                 "global_load_ushort %6, %8, %9 offset:192\n\tglobal_load_ushort %7, %8, %9 offset:224"
                 : "=&v"(wq[0]), "=&v"(wq[1]), "=&v"(wq[2]), "=&v"(wq[3]), "=&v"(wq[4]), "=&v"(wq[5]), "=&v"(wq[6]), "=&v"(wq[7])
                 : "v"(vo), "s"(wph) : "memory");
  }
  u32x4 X[16];
  {
    const unsigned voff = (unsigned)((lane >> 3) * 1024 + (lane & 7) * 16);
    const char* xb = P.x + (p * 128 + 16 * w) * 1024;
#pragma unroll
    for (int i = 0; i < 16; ++i) {
      asm volatile("global_load_dwordx4 %0, %1, %2 offset:%3" : "=v"(X[i]) : "v"(voff), "s"(xb + (i & 1) * 8192), "i"((i >> 1) * 128) : "memory");
    }
  }
  {
    const char* cb = P.code;
    const int ln = w * 64 + lane;
    const unsigned vo = (unsigned)(ln < CODE_LINES ? ln : CODE_LINES - 1) * 128;
    asm volatile("s_mov_b32 m0, %2\n\ts_nop 0\n\tglobal_load_lds_dword %0, %1" ::"v"(vo), "s"(cb), "s"(SCR + w * 256) : "memory");
  }
  STAMP(16);
  asm volatile("s_barrier" ::: "memory");
  const int oct = w & 3;
  const int dr = lane >> 3;
  const int dsw = (lane & 7) ^ ((((dr >> 1) & 1) | ((oct & 1) << 1)) << 1);
  const unsigned vq = (unsigned)((8 * oct + dr) * 3072 + dsw * 16), vg = (unsigned)((8 * oct + dr) * 3584 + dsw * 16);
  const char* sp[5];
  const char* vp[2];
  unsigned vo[5]; int pitch[5]; unsigned lofs[5];
  {
    const char* gb[5];
    if (TA) { gb[0] = P.wqkv + (512 + 64 * ab) * 2; gb[1] = P.wqkv + (1024 + 64 * ab) * 2; gb[2] = P.wg + (16 + ab) * 128; gb[3] = P.wu + (16 + ab) * 128; gb[4] = gb[3]; }
    else { gb[0] = P.wqkv + 64 * ab * 2; gb[1] = P.wg + (2 * ab) * 128; gb[2] = P.wu + (2 * ab) * 128; gb[3] = P.wg + (2 * ab + 1) * 128; gb[4] = P.wu + (2 * ab + 1) * 128; }
#pragma unroll
    for (int m = 0; m < 5; ++m) {
      const int n0 = 8 * m, n1 = 8 * m + 4;
      const int G0 = (n0 % 20) / 4, G1 = (n1 % 20) / 4, S0 = n0 / 20, S1 = n1 / 20;
      const bool q0 = G0 == 0 || (TA && G0 == 1), q1 = G1 == 0 || (TA && G1 == 1);
      pitch[m] = wm ? (q1 ? 3072 : 3584) : (q0 ? 3072 : 3584);
      sp[m] = (wm ? gb[G1] : gb[G0]) + (wm ? S1 : S0) * 32 * pitch[m];
      vo[m] = wm ? (q1 ? vq : vg) : (q0 ? vq : vg);
      lofs[m] = (wm ? G1 : G0) * 4096 + oct * 1024;
    }
    if (TA) {
      const char* hp = (dsw < 4 ? P.wg : P.wu) + (24 + (ab >> 1)) * 128 + (ab & 1) * 64 + (8 * oct + dr) * 3584 + (dsw & 3) * 16;
      vp[0] = wm ? sp[2] + vo[2] : hp;
      vp[1] = wm ? hp + 32 * 3584 : sp[4] + vo[4];
    }
  }
  auto wpiece = [&](int D, int m) {
    const unsigned sa = ((2 * D + 4) % NWS) * WSLOT, sb = ((2 * D + 5) % NWS) * WSLOT;
    unsigned lb = lofs[m], hw = wm;
    asm volatile("" : "+s"(lb), "+s"(hw));
    const unsigned lds = lb + (m < 2 ? sa : m > 2 ? sb : sa + hw * (sb - sa));
    if (TA && (m == 2 || m == 4)) { dma_v(vp[m == 4], lds); vp[m == 4] += 64 * pitch[m]; }
    else { dma_s(vo[m], sp[m], lds); sp[m] += 64 * pitch[m]; }
  };
  float s1[2] = {0.f, 0.f}, s2[2] = {0.f, 0.f};
  const b16x2 ones2 = __builtin_bit_cast(b16x2, 0x3f803f80u);
#define XWAIT(N, i) asm volatile("s_waitcnt vmcnt(%4)" : "+v"(X[i]), "+v"(X[i + 1]), "+v"(X[i + 2]), "+v"(X[i + 3]) : "i"(N) : "memory")
#pragma unroll
  for (int q = 0; q < 4; ++q) {
#pragma unroll
    for (int n = 3 * q; n < 3 * q + 3; ++n) wpiece(n / 5, n % 5);
    if (q == 0) { XWAIT(16, 0); asm volatile("" : "+v"(nonce), "+v"(wq[0]), "+v"(wq[1]), "+v"(wq[2]), "+v"(wq[3]), "+v"(wq[4]), "+v"(wq[5]), "+v"(wq[6]), "+v"(wq[7])); }
    if (q == 1) XWAIT(15, 4);
    if (q == 2) XWAIT(14, 8);
    if (q == 3) XWAIT(13, 12);
    STAMP(17 + q);
#pragma unroll
    for (int i = 4 * q; i < 4 * q + 4; ++i)
#pragma unroll
      for (int e = 0; e < 4; ++e) {
        const unsigned xv = X[i][e];
        const b16x2 v = __builtin_bit_cast(b16x2, xv);
        s1[i & 1] = __builtin_amdgcn_fdot2_f32_bf16(v, ones2, s1[i & 1], false);
        s2[i & 1] = __builtin_amdgcn_fdot2_f32_bf16(v, v, s2[i & 1], false);
      }
    PIN();
  }
  STAMP(0);
  const u64 nn = ((u64)(unsigned)__builtin_amdgcn_readfirstlane(nonce[1]) << 32) | (unsigned)__builtin_amdgcn_readfirstlane(nonce[0]);
  const u64 tag = nn * 6364136223846793005ull + 1442695040888963407ull;
  tag_out = tag;
  u32x4 wphi = {wq[0] | (wq[1] << 16), wq[2] | (wq[3] << 16), wq[4] | (wq[5] << 16), wq[6] | (wq[7] << 16)};
  wphi ^= (t32 & 4) ? 0x80008000u : 0u;
  asm volatile("" : "+v"(wphi));
  float na[2], nb[2];
#pragma unroll
  for (int r = 0; r < 2; ++r) {
    float a = s1[r], b = s2[r];
    a = dpp_add<0xB1>(a); b = dpp_add<0xB1>(b);
    a = dpp_add<0x4E>(a); b = dpp_add<0x4E>(b);
    a = dpp_add<0x141>(a); b = dpp_add<0x141>(b);
    const float m = a * (1.0f / 512.0f);
    const float var = fmaxf(b * (1.0f / 512.0f) - m * m, 0.0f);
    na[r] = __builtin_amdgcn_rsqf(var + 1e-5f);
    nb[r] = -m * na[r];
  }
  const int xw = XOFF + (16 * w + dr) * 128 + (((lane & 7) ^ ((dr >> 1) & 3)) * 16);
  auto xaddr = [&](int i) { return ((xw + (i & 1) * 1024) ^ ((i & 1) * 64)) + (((i >> 1) + 1) & 1) * XSLOT; };
  auto xwrite = [&](int i) {
    u32x4 o;
#pragma unroll
    for (int e = 0; e < 4; ++e) o[e] = norm2(X[i][e], na[i & 1], nb[i & 1]);
    lds_w128(xaddr(i), o);
  };
  const int q4 = t16 >> 2, p4 = lane & 3;
  const int fsw = ((q4 >> 1) & 1) | ((g & 1) << 1);
  const int trb = g * 1024 + q4 * 128 + 8 * p4;
  const int tr0 = trb + ((wn ^ fsw) * 32);
  const int trg = trb + (((wn >> 1) ^ fsw) * 32) + 4 * 4096, tru = trb + (((2 + (wn >> 1)) ^ fsw) * 32) + 4 * 4096;
  const int xr = XOFF + (64 * wm + t16) * 128 + ((g ^ (t16 >> 1)) * 16);
  const int pr = TA ? (wn & 1) : 0;
  const int xrA = xr + pr * 4096, xrB = xr + (1 - pr) * 4096;
  const unsigned v2 = w2_voff(lane, wm, ct64);
  constexpr int NWF = TA ? 6 : 5, NRD = 2 * NWF + 4;
  u32x2 wf[2][NWF][2];
  u32x4 xf[2][4];
  auto fread = [&](int s, int r) {
    const int slot = ((s + 4) % NWS) * WSLOT, b = s & 1, xs = (((s >> 1) + 1) & 1) * XSLOT;
    if (r < 4) xf[b][r] = lds_r128(((r < 2 ? xrA : xrB) ^ (b * 64)) + xs + (r & 1) * 2048);
    else {
      const int G = (r - 4) >> 1, t = (r - 4) & 1;
      wf[b][G][t] = tr8(((TA && G == 4) ? trg : (TA && G == 5) ? tru : tr0 + G * 4096) + slot + t * 512);
    }
  };
  f32x4 acc[5][4];
#pragma unroll
  for (int a = 0; a < 5; ++a)
#pragma unroll
    for (int b = 0; b < 4; ++b) acc[a][b] = (f32x4){0.f, 0.f, 0.f, 0.f};
  xwrite(0); xwrite(1);
  wait_vm_bar<9>();
  STAMP(1);
#pragma unroll
  for (int r = 0; r < NRD; ++r) fread(0, r);
  xwrite(2);
#pragma unroll
  for (int s = 0; s < 16; ++s) {
    if (s <= 10) wait_vm_bar<7>(); else if (s <= 13) wait_vm_bar<8>(); else if (s == 14) wait_vm_bar<10>();
    PIN();
    const int b = s & 1;
    u32x4 xo;
#pragma unroll
    for (int k = 0; k < 20; ++k) {
      const int G = k >> 2, rt = k & 3;
      if (TA && G == 4) {
        const int gi = 4 + (rt >> 1);
        acc[4][rt] = mfma16((u32x4){wf[b][gi][0][0], wf[b][gi][0][1], wf[b][gi][1][0], wf[b][gi][1][1]}, xf[b][rt & 1], acc[4][rt]);
      } else {
        const u32x4 wv = {wf[b][G][0][0], wf[b][G][0][1], wf[b][G][1][0], wf[b][G][1][1]};
        acc[G][rt] = (TA && G == 1) ? mfma16(xf[b][rt], wv, acc[G][rt]) : mfma16(wv, xf[b][rt], acc[G][rt]);
      }
      asm volatile("" : "+v"(acc[G][rt]));
      PIN();
      if (s <= 10) {
        if (s & 1) { if (k == 0) wpiece((s + 5) / 2, 0); if (k == 6) wpiece((s + 5) / 2, 1); }
        else { if (k == 0) wpiece((s + 4) / 2, 2); if (k == 5) wpiece((s + 4) / 2, 3); if (k == 10) wpiece((s + 4) / 2, 4); }
      }
      if (s == 10 && k == 15) w2_piece(P, 17, wn, wm, v2);
      if (s == 11) { if (k == 0) w2_piece(P, 0, wn, wm, v2); if (k == 6) w2_piece(P, 1, wn, wm, v2); }
      if (s == 12) { if (k == 0) w2_piece(P, 2, wn, wm, v2); if (k == 5) w2_piece(P, 3, wn, wm, v2); if (k == 10) w2_piece(P, 4, wn, wm, v2); }
      if (s == 13) { if (k == 0) w2_piece(P, 5, wn, wm, v2); if (k == 5) w2_piece(P, 6, wn, wm, v2); if (k == 10) w2_piece(P, 15, wn, wm, v2); if (k == 15) w2_piece(P, 16, wn, wm, v2); }
      if (s == 14) { if (k == 0) w2_piece(P, 7, wn, wm, v2); if (k == 5) w2_piece(P, 8, wn, wm, v2); if (k == 10) w2_piece(P, 9, wn, wm, v2); }
      if (s < 15 && k >= 1 && k - 1 < NRD) fread(s + 1, k - 1);
      if (s + 3 < 16) {
        const int i = s + 3;
        if ((k & 3) == 3 && k < 16) xo[k >> 2] = norm2(X[i][k >> 2], na[i & 1], nb[i & 1]);
        if (k == 16) lds_w128(xaddr(i), xo);
      }
      if (s == 15 && k == 0) {
        bar();
#pragma unroll
        for (int i = 10; i < 15; ++i) w2_piece(P, i, wn, wm, v2);
      }
      PIN();
    }
  }
  STAMP(2);
  const unsigned abase = WS_AB + (unsigned)(p * 4 + wm * 2) * (144 * 1024);
  const unsigned l16 = lane * 16;
  const int scr = SCR + w * 2048, scr_o = SCR + (w ^ 4) * 2048;
  const unsigned fh = WS_FH + ((((p * 2) * 4 + (ab >> 1)) * 2 + wm) * 16 + (ab & 1) * 4 + wn) * 128;
  if (TA) {
    f32x16 kv = zero16();
    const int iw = scr + t32 * 64, sw = (t32 >> 1) & 3;
    const int rr = 8 * h + q4, uu = (p4 & 1) * 4 + 2 * (g & 1) + (p4 >> 1);
    const int ir0 = scr + rr * 64 + (((uu >> 1) ^ ((rr >> 1) & 3)) * 16) + (uu & 1) * 8;
    const int ir1 = scr + (rr + 4) * 64 + (((uu >> 1) ^ (((rr + 4) >> 1) & 3)) * 16) + (uu & 1) * 8;
#pragma unroll
    for (int pp = 0; pp < 2; ++pp) {
      u32x4 f0, f1;
      softmax16(mfma32(wphi, mk_frag(pk4(acc[0][2 * pp]), pk4(acc[0][2 * pp + 1])), zero16()), f0, f1);
      lds_w128(iw + (((2 * h) ^ sw) * 16), f0);
      lds_w128(iw + (((2 * h + 1) ^ sw) * 16), f1);
#pragma unroll
      for (int e = 0; e < 2; ++e) {
        const u32x2 a0 = tr8(ir0 + e * 1024), a1 = tr8(ir1 + e * 1024);
        const u32x4 vb = mk_frag(pk4(acc[1][2 * pp + e]), (u32x2){0x3f803f80u, 0x3f803f80u});
        kv = mfma32((u32x4){a0[0], a0[1], a1[0], a1[1]}, vb, kv);
      }
    }
    STAMP(11);
    f32x4 k0, k1;
    if (wm == 0) {
      lds_wf(scr + l16, (f32x4){kv[8], kv[9], kv[10], kv[11]}); lds_wf(scr + 1024 + l16, (f32x4){kv[12], kv[13], kv[14], kv[15]});
      k0 = (f32x4){kv[0], kv[1], kv[2], kv[3]}; k1 = (f32x4){kv[4], kv[5], kv[6], kv[7]};
    } else {
      lds_wf(scr + l16, (f32x4){kv[0], kv[1], kv[2], kv[3]}); lds_wf(scr + 1024 + l16, (f32x4){kv[4], kv[5], kv[6], kv[7]});
      k0 = (f32x4){kv[8], kv[9], kv[10], kv[11]}; k1 = (f32x4){kv[12], kv[13], kv[14], kv[15]};
    }
    bar();
    k0 += lds_rf(scr_o + l16); k1 += lds_rf(scr_o + 1024 + l16);
    {
      const u32x2 a = pk4(k0), b = pk4(k1);
      const unsigned id = (ab * 4 + wn) * 16 + p;
      const unsigned off = t32 < 16 ? WS_KVP + id * 1024 + wm * 512 + h * 256 + t16 * 16 : WS_ZP + id * 64 + h * 32 + wm * 16;
      if (t32 <= 16) store_sc1(P.ws, off, (u32x4){a[0], a[1], b[0], b[1]});
    }
    asm volatile("" ::: "memory");
    const u32x4 fr0 = mk_frag(glu4(acc[2][0], acc[3][0]), glu4(acc[2][1], acc[3][1]));
    const u32x4 fr1 = mk_frag(glu4(acc[2][2], acc[3][2]), glu4(acc[2][3], acc[3][3]));
    store_sc1(P.ws + (abase + pr * (144 * 1024) + (64 + 4 * ab + wn) * 1024), l16, fr0);
    store_sc1(P.ws + (abase + (1 - pr) * (144 * 1024) + (64 + 4 * ab + wn) * 1024), l16, fr1);
    wait_vm<2>();
    set_flag(P.ws, WS_FKV + (((ab * 4 + wn) * 16 + p) * 2 + wm) * 128, tag, lane);
    STAMP(3);
    const u32x4 fr2 = mk_frag(glu4(acc[4][0], acc[4][2]), glu4(acc[4][1], acc[4][3]));
    store_sc1(P.ws + (abase + pr * (144 * 1024) + (96 + 2 * ab + (wn >> 1)) * 1024), l16, fr2);
    bar();
    w2_regs(wr, lane, wn);
    wait_vm<0>();
    set_flag(P.ws, fh + 8 * 128, tag, lane);
    STAMP(4);
    bar();
#pragma unroll
    for (int i = 18; i < 27; ++i) w2_piece(P, i, wn, wm, v2);
  } else {
#pragma unroll
    for (int h2 = 0; h2 < 2; ++h2)
#pragma unroll
      for (int pp = 0; pp < 2; ++pp) {
        const u32x4 fr4 = mk_frag(glu4(acc[1 + 2 * h2][2 * pp], acc[2 + 2 * h2][2 * pp]), glu4(acc[1 + 2 * h2][2 * pp + 1], acc[2 + 2 * h2][2 * pp + 1]));
        store_sc1(P.ws + (abase + pp * (144 * 1024) + (8 * ab + 4 * h2 + wn) * 1024), l16, fr4);
      }
    STAMP(11);
    wait_vm_bar<4>();
    w2_regs(wr, lane, wn);
    u32x4 ph[2][2];
#pragma unroll
    for (int pp = 0; pp < 2; ++pp)
      softmax16(mfma32(wphi, mk_frag(pk4(acc[0][2 * pp]), pk4(acc[0][2 * pp + 1])), zero16()), ph[pp][0], ph[pp][1]);
    KEEP(ph[0][0]); KEEP(ph[0][1]); KEEP(ph[1][0]); KEEP(ph[1][1]);
    wait_vm<0>();
    set_flag(P.ws, fh, tag, lane);
    STAMP(3);
    bar();
    poll_flags(P.ws, WS_FKV + (ab * 4 + wn) * 32 * 128, 32, tag, lane);
    STAMP(12);
    u32x4 part[8], zq;
    {
      const char* kb = P.ws + WS_KVP + ((ab * 4 + wn) * 16 + 8 * wm) * 1024;
      const char* zb = P.ws + WS_ZP + (ab * 4 + wn) * 1024;
      const unsigned vk = (g & 1) * 4096 + h * 256 + t16 * 16, vz = t16 * 64 + g * 16;
      asm volatile("global_load_dwordx4 %0, %1, %2 sc1" : "=v"(zq) : "v"(vz), "s"(zb) : "memory");
#pragma unroll
      for (int i = 0; i < 8; ++i) asm volatile("global_load_dwordx4 %0, %1, %2 offset:%3 sc1" : "=v"(part[i]) : "v"(vk), "s"(kb), "i"(i * 512) : "memory");
    }
#pragma unroll
    for (int i = 18; i < 27; ++i) w2_piece(P, i, wn, wm, v2);
    STAMP(21);
    asm volatile("s_waitcnt vmcnt(17)" : "+v"(zq)::"memory");
    STAMP(22);
    float z[8];
#pragma unroll
    for (int e = 0; e < 4; ++e) { z[2 * e] = lo16(zq[e]); z[2 * e + 1] = hi16(zq[e]); }
#pragma unroll
    for (int e = 0; e < 8; ++e) { z[e] = dpp_add<0xB1>(z[e]); z[e] = dpp_add<0x4E>(z[e]); z[e] = dpp_add<0x141>(z[e]); z[e] = dpp_add<0x140>(z[e]); }
    u32x4 zp = {pk2(z[0], z[1]), pk2(z[2], z[3]), pk2(z[4], z[5]), pk2(z[6], z[7])};
    KEEP(zp);
    asm volatile("s_waitcnt vmcnt(9)" : "+v"(part[0]), "+v"(part[1]), "+v"(part[2]), "+v"(part[3]), "+v"(part[4]), "+v"(part[5]), "+v"(part[6]), "+v"(part[7])::"memory");
    STAMP(23);
    f32x4 k0, k1;
#pragma unroll
    for (int e = 0; e < 8; ++e) {
      float a = 0.f, b = 0.f;
#pragma unroll
      for (int i = 0; i < 4; ++i) {
        const unsigned ua = part[2 * i][e >> 1], ub = part[2 * i + 1][e >> 1];
        a += (e & 1) ? hi16(ua) : lo16(ua); b += (e & 1) ? hi16(ub) : lo16(ub);
      }
      auto r = __builtin_amdgcn_permlane16_swap(ubits(a), ubits(b), false, false);
      const float v = fbits(r[0]) + fbits(r[1]);
      if (e < 4) k0[e] = v; else k1[e - 4] = v;
    }
    lds_wf(scr + l16, k0); lds_wf(scr + 1024 + l16, k1);
    bar();
    k0 += lds_rf(scr_o + l16); k1 += lds_rf(scr_o + 1024 + l16);
    u32x4 a0, a1;
    {
      const u32x2 ka = pk4(k0), kb = pk4(k1);
      const unsigned kp[4] = {ka[0], ka[1], kb[0], kb[1]};
#pragma unroll
      for (int e = 0; e < 4; ++e) {
        auto r = __builtin_amdgcn_permlane16_swap(kp[e], zp[e], false, false);
        a0[e] = r[0]; a1[e] = r[1];
      }
    }
#pragma unroll
    for (int pp = 0; pp < 2; ++pp) {
      const f32x16 nd = mfma32(a1, ph[pp][1], mfma32(a0, ph[pp][0], zero16()));
      const float r = __builtin_amdgcn_rcpf(nd[8] + 1e-6f);
      auto s0 = __builtin_amdgcn_permlane32_swap(pk2(nd[0] * r, nd[1] * r), pk2(nd[4] * r, nd[5] * r), false, false);
      auto s1 = __builtin_amdgcn_permlane32_swap(pk2(nd[2] * r, nd[3] * r), pk2(nd[6] * r, nd[7] * r), false, false);
      store_sc1(P.ws + (abase + pp * (144 * 1024) + (112 + 4 * ab + wn) * 1024), l16, (u32x4){s0[0], s1[0], s0[1], s1[1]});
    }
    STAMP(24);
    wait_vm<0>();
    set_flag(P.ws, fh + 4 * 2 * 16 * 128, tag, lane);
    STAMP(4);
  }
}

__global__ __launch_bounds__(512, 2) void fused_block(const char* x, const char* wqkv, const char* wphi, const char* wo, const char* wg,
                                                      const char* wu, const char* wd, char* out, char* ws) {
  u64 pc; asm volatile("s_getpc_b64 %0" : "=s"(pc));
  const int lane = threadIdx.x & 63;
  int w = __builtin_amdgcn_readfirstlane(threadIdx.x >> 6);
  const int b = blockIdx.x, xcd = b & 7, bi = b >> 3;
  const Ptrs P = {x, wqkv, wphi, wo, wg, wu, wd, out, ws, (const char*)(pc & ~(u64)0xff)};
  const int p = (xcd & 1) * 8 + (bi & 7), jj = bi >> 3, ab = (xcd >> 1) * 2 + (jj & 1);
  int t64 = xcd * 4 + (bi >> 3), ct64 = bi & 7;
  STAMP(7);
  u64 tag;
  u32x4 wr[18][2];
  if (jj < 2) phase1<true>(P, lane, w, p, ab, ct64, tag, wr STAMP_PASS);
  else phase1<false>(P, lane, w, p, ab, ct64, tag, wr STAMP_PASS);

  asm volatile("" : "+s"(w), "+s"(t64), "+s"(ct64));
  const int kq = w & 3, rh = w >> 2;
  const int h = lane >> 5, q4 = (lane & 15) >> 2, blk = (lane >> 4) & 1;
  const int w2r = kq * 2048 + (8 * h + q4) * 128 + 8 * (lane & 3);
  const int w2a = w2r + ((blk ^ (q4 & 2)) * 32), w2b = w2r + (((2 + blk) ^ (q4 & 2)) * 32);
  const unsigned v2 = w2_voff(lane, rh, ct64);
  const char* ab0 = ws + WS_AB + (unsigned)(t64 * 2 + rh) * (144 * 1024);
  const unsigned l16 = lane * 16;
  STAMP(5);
  u32x4 res;
  {
    const char* xs = x + (t64 * 64 + rh * 32) * 1024 + ct64 * 128 + kq * 32;
    const unsigned vo = (lane & 31) * 1024 + h * 16;
    asm volatile("global_load_dwordx4 %0, %1, %2" : "=v"(res) : "v"(vo), "s"(xs) : "memory");
  }
  f32x16 acc0 = zero16(), acc1 = zero16();
  u32x4 af[16];
  auto aload = [&](int i) {
    int q = kq;
    asm volatile("" : "+s"(q));
    const int i4 = i & ~3;
    const int ks = i4 < 16 ? 16 * q + i4 : i4 < 24 ? 64 + 8 * q + (i4 - 16) : i4 < 28 ? 96 + 4 * q : 112 + 8 * q + (i4 - 28);
    const char* s = ab0 + ks * 1024;
    asm volatile("global_load_dwordx4 %0, %1, %2 offset:%3 sc1" : "=v"(af[i & 15]) : "v"(l16), "s"(s), "i"((i & 3) * 1024) : "memory");
  };
  const unsigned fh = WS_FH + ((((t64 >> 1) * 2) * 4 + kq) * 2 + (t64 & 1)) * 16 * 128;
  poll_flags(ws, fh, 16, tag, lane);
  asm volatile("" : "+v"(res));
  STAMP(13);
#pragma unroll
  for (int i = 0; i < 16; ++i) aload(i);
#pragma unroll
  for (int i = 27; i < 36; ++i) w2_piece(P, i, kq, rh, v2);
  u32x2 fl;
  u32x4 wn0, wn1;
#pragma unroll
  for (int i = 0; i < 36; ++i) {
    if (i == 1) STAMP(25);
    if (i == 12) {
      STAMP(26);
      asm volatile("s_waitcnt vmcnt(6)" : "+v"(fl)::"memory");
      const u64 v = ((u64)fl[1] << 32) | fl[0];
      if (!__all(v == tag)) poll_flags(ws, fh + 4 * 2 * 16 * 128, 8, tag, lane);
    }
    if (i == 12) STAMP(27);
    if (i == 28) STAMP(28);
    if (i == 17 || i == 30) bar();
    if (i < 16) asm volatile("s_waitcnt vmcnt(24)" : "+v"(af[i & 15])::"memory");
    else if (i < 20) asm volatile("s_waitcnt vmcnt(15)" : "+v"(af[i & 15])::"memory");
    else asm volatile("s_waitcnt vmcnt(%1)" : "+v"(af[i & 15]) : "i"(35 - i) : "memory");
    const u32x4 a = af[i & 15];
    const u32x4 w0 = i < 18 ? wr[i < 18 ? i : 0][0] : wn0, w1 = i < 18 ? wr[i < 18 ? i : 0][1] : wn1;
    acc0 = mfma32(w0, a, acc0);
    asm volatile("" : "+v"(acc0));
    if (i >= 17 && i < 35) wn0 = tr16(w2a + (i - 17) * 8192, 512);
    acc1 = mfma32(w1, a, acc1);
    asm volatile("" : "+v"(acc1));
    if (i >= 17 && i < 35) wn1 = tr16(w2b + (i - 17) * 8192, 512);
    PIN();
    if (i + 16 < 36) aload(i + 16);
    if (i == 5) {
      const char* fb = ws + fh + 4 * 2 * 16 * 128;
      const unsigned vo = (lane & 7) * 128;
      asm volatile("global_load_dwordx2 %0, %1, %2 sc1" : "=v"(fl) : "v"(vo), "s"(fb) : "memory");
    }
    PIN();
  }
  STAMP(6);
  float v[8];
#pragma unroll
  for (int d = 0; d < 4; ++d) {
    const f32x16& s = d < 2 ? acc0 : acc1;
    const int o = (d & 1) * 8;
    const int dst = ((rh * 4 + d) * 4 + kq) * 2048 + l16;
    if (d != kq) {
      lds_wf(dst, (f32x4){s[o], s[o + 1], s[o + 2], s[o + 3]});
      lds_wf(dst + 1024, (f32x4){s[o + 4], s[o + 5], s[o + 6], s[o + 7]});
    } else {
#pragma unroll
      for (int e = 0; e < 8; ++e) v[e] = s[o + e];
    }
  }
  bar();
#pragma unroll
  for (int n = 1; n < 4; ++n) {
    const int src = ((rh * 4 + kq) * 4 + (kq ^ n)) * 2048 + l16;
    const f32x4 a = lds_rf(src), c = lds_rf(src + 1024);
#pragma unroll
    for (int e = 0; e < 4; ++e) { v[e] += a[e]; v[4 + e] += c[e]; }
  }
#pragma unroll
  for (int e = 0; e < 4; ++e) {
    auto r = __builtin_amdgcn_permlane32_swap(ubits(v[e]), ubits(v[4 + e]), false, false);
    v[e] = fbits(r[0]); v[4 + e] = fbits(r[1]);
  }
  u32x4 o;
#pragma unroll
  for (int e = 0; e < 4; ++e) o[e] = pk2(v[2 * e] + lo16(res[e]), v[2 * e + 1] + hi16(res[e]));
  *(GAS u32x4*)(out + (t64 * 64 + rh * 32 + (lane & 31)) * 1024 + ct64 * 128 + kq * 32 + h * 16) = o;
  if (b == 0) {
    if (w < 4) {
      const int id = w * 64 + lane, pp = id >> 4, ty = (id >> 3) & 1, aa = id & 7;
      GAS u64* f = (GAS u64*)(ws + WS_FH + ((((pp * 2) * 4 + (aa >> 1)) * 2) * 16 + ty * 8 + (aa & 1) * 4) * 128);
#pragma unroll 1
      for (unsigned spins = 0; spins < SPIN_CAP; ++spins) {
        if (__all(__hip_atomic_load(f, __ATOMIC_RELAXED, __HIP_MEMORY_SCOPE_AGENT) == tag)) break;
        __builtin_amdgcn_s_sleep(2);
      }
    }
    bar();
    if (threadIdx.x == 0) __hip_atomic_store((GAS u64*)(ws + WS_NONCE), tag, __ATOMIC_RELAXED, __HIP_MEMORY_SCOPE_AGENT);
  }
}

extern "C" void kernel_launch(void* const* d_in, const int* in_sizes, int n_in, void* d_out, int out_size, void* d_ws, size_t ws_size,
                              hipStream_t stream) {
  (void)hipFuncSetAttribute((const void*)fused_block, hipFuncAttributeMaxDynamicSharedMemorySize, LDS_BYTES);
  fused_block<<<256, 512, LDS_BYTES, stream>>>((const char*)d_in[0], (const char*)d_in[1], (const char*)d_in[2], (const char*)d_in[3],
                                               (const char*)d_in[5], (const char*)d_in[6], (const char*)d_in[7], (char*)d_out, (char*)d_ws);
}
```
